```python
import torch

def _softmax(x, axis=-1):
    return torch.softmax(x, dim=axis)
def _gelu(x):
    return torch.nn.functional.gelu(x, approximate="tanh")
def _silu(x):
    return torch.nn.functional.silu(x)
def _sigmoid(x):
    return torch.sigmoid(x)
def _layernorm(x, axis=-1, eps=1e-5):
    m = x.mean(dim=axis, keepdim=True)
    v = x.var(dim=axis, keepdim=True, unbiased=False)
    return (x - m) / torch.sqrt(v + eps)
def _scatter_add(tgt, idx, src, axis=0):
    return tgt.clone().index_add_(axis, idx, src)
def _segment_sum(src, seg, n):
    out = torch.zeros((n,) + src.shape[1:], dtype=src.dtype)
    return out.index_add_(0, seg, src)
def _pad(x, pw):
    flat = tuple(v for pair in reversed(pw) for v in pair)
    return torch.nn.functional.pad(x, flat)

kernel_name = 'standalone__gqa_softmax__near_ridge'

def setup_inputs():

    _w = (lambda a: (lambda t: t.to(torch.bfloat16) if t.is_floating_point() else t)(torch.as_tensor(a)))
    B, Hq, Hkv, Sq, Sk, D = 8, 32, 8, 320, 320, 128
    return {
        "q": _w((lambda *s: torch.randn(*s, dtype=torch.bfloat16) * 0.8)(B, Hq, Sq, D)),
        "k": _w((lambda *s: torch.randn(*s, dtype=torch.bfloat16) * 0.8)(B, Hkv, Sk, D)),
        "v": _w((lambda *s: torch.randn(*s, dtype=torch.bfloat16) * 0.05)(B, Hkv, Sk, D)),
    }

def reference(q, k, v):
    B, Hq, Sq, D = q.shape
    Hkv = k.shape[1]
    rep = Hq // Hkv
    k = (lambda x, n, axis: torch.repeat_interleave(x, n, dim=axis))(k, rep, axis=1)
    v = (lambda x, n, axis: torch.repeat_interleave(x, n, dim=axis))(v, rep, axis=1)
    scale = D ** -0.5
    scores = torch.einsum("bhqd,bhkd->bhqk", q, k) * scale
    p = _softmax(scores, axis=-1)
    return torch.einsum("bhqk,bhkd->bhqd", p, v)
```

```cpp
#include <hip/hip_runtime.h>
#include <hip/hip_bf16.h>

typedef __bf16   bf16x8 __attribute__((ext_vector_type(8)));
typedef __bf16   bf16x2 __attribute__((ext_vector_type(2)));
typedef short    s16x4  __attribute__((ext_vector_type(4)));
typedef short    s16x8  __attribute__((ext_vector_type(8)));
typedef float    f32x16 __attribute__((ext_vector_type(16)));
typedef float    f32x4  __attribute__((ext_vector_type(4)));
typedef float    f32x2  __attribute__((ext_vector_type(2)));
typedef unsigned u32x4  __attribute__((ext_vector_type(4)));
#define DI __device__ __forceinline__
#define LDS_AS __attribute__((address_space(3)))

constexpr int NB = 8, HQ = 32, HKV = 8, SEQ = 320, HD = 128;
constexpr unsigned HEAD_BYTES = SEQ * HD * 2;
constexpr unsigned VOFF = HEAD_BYTES;
constexpr float CSC = 0.088388347648318440f * 1.4426950408889634f;

DI unsigned cvtpk(float lo, float hi) {
  f32x2 v = {lo, hi}; bf16x2 b = __builtin_convertvector(v, bf16x2); return __builtin_bit_cast(unsigned, b);
}
DI float xhalf_max(float x) {
  auto r = __builtin_amdgcn_permlane32_swap(__float_as_uint(x), __float_as_uint(x), false, false);
  return fmaxf(__uint_as_float(r[0]), __uint_as_float(r[1]));
}
DI float xhalf_sum(float x) {
  auto r = __builtin_amdgcn_permlane32_swap(__float_as_uint(x), __float_as_uint(x), false, false);
  return __uint_as_float(r[0]) + __uint_as_float(r[1]);
}
DI void dma16(unsigned m0v, unsigned voff, u32x4 rsrc, unsigned soff) {
  asm volatile("s_mov_b32 m0, %0\n\ts_nop 0\n\tbuffer_load_dwordx4 %1, %2, %3 offen lds"
               :: "s"(m0v), "v"(voff), "s"(rsrc), "s"(soff) : "memory");
}
#define QLOAD(i) asm volatile("buffer_load_dwordx4 %0, %1, %2, %3 offen offset:" #i : "=v"(q[(i) / 32]) : "v"(qvoff), "s"(rq), "s"(qsoff) : "memory")
DI void load_q(bf16x8 (&q)[8], unsigned qvoff, u32x4 rq, unsigned qsoff) {
  QLOAD(0); QLOAD(32); QLOAD(64); QLOAD(96); QLOAD(128); QLOAD(160); QLOAD(192); QLOAD(224);
}
#define WAIT_Q(N) asm volatile("s_waitcnt vmcnt(" #N ")" : "+v"(q[0]), "+v"(q[1]), "+v"(q[2]), "+v"(q[3]), "+v"(q[4]), "+v"(q[5]), "+v"(q[6]), "+v"(q[7]) :: "memory")
#define BARRIER() asm volatile("s_barrier" ::: "memory")

template <int NKB>
DI void qk_phase(f32x16 (&s)[NKB], const bf16x8 (&q)[8], const LDS_AS char* lds, unsigned kaddr) {
#pragma unroll
  for (int kb = 0; kb < NKB; ++kb) {
    f32x16 acc = {};
#pragma unroll
    for (int st = 0; st < 8; ++st) {
      const bf16x8 a = *(const LDS_AS bf16x8*)(lds + ((kaddr ^ (unsigned)(st << 5)) + (unsigned)(kb * 8192)));
      acc = __builtin_amdgcn_mfma_f32_32x32x16_bf16(a, q[st], acc, 0, 0, 0);
    }
    s[kb] = acc;
  }
}

DI void exp_pack(const f32x16& s, float mc, float (&lp)[4], bf16x8 (&pb)[2]) {
  float p[16];
#pragma unroll
  for (int r = 0; r < 16; ++r) { p[r] = __builtin_amdgcn_exp2f(fmaf(s[r], CSC, mc)); lp[r & 3] += p[r]; }
#pragma unroll
  for (int ks = 0; ks < 2; ++ks) {
    u32x4 pw;
#pragma unroll
    for (int j = 0; j < 4; ++j) pw[j] = cvtpk(p[8 * ks + 2 * j], p[8 * ks + 2 * j + 1]);
    pb[ks] = __builtin_bit_cast(bf16x8, pw);
  }
  asm volatile("" : "+v"(lp[0]), "+v"(lp[1]), "+v"(lp[2]), "+v"(lp[3]));
}

template <int NKB, int HOOK_KB, class F>
DI void softmax_pv(f32x16 (&s)[NKB], f32x16 (&o)[4], float& m_out, float& l_out, const LDS_AS char* lds, const unsigned (&vaddr)[4], F hook) {
  float m = s[0][0];
#pragma unroll
  for (int kb = 0; kb < NKB; ++kb)
#pragma unroll
    for (int r = 0; r < 16; ++r) m = fmaxf(m, s[kb][r]);
  m = xhalf_max(m);
  const float mc = -m * CSC;
  float lp[4] = {0.f, 0.f, 0.f, 0.f};
#pragma unroll
  for (int t = 0; t < 4; ++t) o[t] = f32x16{};
  bf16x8 pb[2], pn[2];
  exp_pack(s[0], mc, lp, pb);
#pragma unroll
  for (int kb = 0; kb < NKB; ++kb) {
    __builtin_amdgcn_sched_barrier(0);
    if (kb == HOOK_KB) hook();
    if (kb + 1 < NKB) exp_pack(s[kb + 1], mc, lp, pn);
#pragma unroll
    for (int ks = 0; ks < 2; ++ks) {
#pragma unroll
      for (int t = 0; t < 4; ++t) {
        const unsigned off = (unsigned)((32 * kb + 16 * ks) * 256);
        const s16x4 lo = __builtin_amdgcn_ds_read_tr16_b64_v4i16((LDS_AS s16x4*)(lds + (vaddr[t] + off)));
        const s16x4 hi = __builtin_amdgcn_ds_read_tr16_b64_v4i16((LDS_AS s16x4*)(lds + (vaddr[t] + off + 2048u)));
        const s16x8 av = __builtin_shufflevector(lo, hi, 0, 1, 2, 3, 4, 5, 6, 7);
        o[t] = __builtin_amdgcn_mfma_f32_32x32x16_bf16(__builtin_bit_cast(bf16x8, av), pb[ks], o[t], 0, 0, 0);
      }
    }
    pb[0] = pn[0]; pb[1] = pn[1];
  }
  __builtin_amdgcn_sched_barrier(0);
  m_out = m;
  l_out = xhalf_sum((lp[0] + lp[1]) + (lp[2] + lp[3]));
}

DI void store_tile(const f32x16& o, float sc, char* rowp, int t) {
#pragma unroll
  for (int hf = 0; hf < 2; ++hf) {
    u32x4 w;
#pragma unroll
    for (int j = 0; j < 4; ++j) w[j] = cvtpk(o[8 * hf + 2 * j] * sc, o[8 * hf + 2 * j + 1] * sc);
    *(u32x4*)(rowp + 64 * t + 32 * hf) = w;
  }
}

__global__ __launch_bounds__(512, 2)
void gqa_attn_kernel(const char* __restrict__ Q, const char* __restrict__ K, const char* __restrict__ V, char* __restrict__ O) {
  __shared__ __attribute__((aligned(16))) char smem[2 * HEAD_BYTES];
  const LDS_AS char* lds = (const LDS_AS char*)smem;
  const unsigned ldsb = (unsigned)(size_t)(LDS_AS char*)smem;
  const unsigned lane = threadIdx.x & 63;
  const unsigned wave = __builtin_amdgcn_readfirstlane(threadIdx.x >> 6);
  const unsigned bid = blockIdx.x, xcd = bid & 7, idx = bid >> 3;
  const unsigned grp = xcd * 8 + (idx >> 2);
  const unsigned qh = grp * 4 + (idx & 3);
  const char* Kg = K + (size_t)grp * HEAD_BYTES;
  const char* Vg = V + (size_t)grp * HEAD_BYTES;
  const char* Qg = Q + (size_t)qh * HEAD_BYTES;
  char* Og = O + (size_t)qh * HEAD_BYTES;
  auto mk = [](const char* p) { u32x4 r; const unsigned long long a = (unsigned long long)p;
    r[0] = (unsigned)a; r[1] = (unsigned)(a >> 32) & 0xffffu; r[2] = HEAD_BYTES; r[3] = 0x00020000u; return r; };
  const u32x4 rk = mk(Kg), rv = mk(Vg), rq = mk(Qg);

  const unsigned r32 = lane & 31, h = lane >> 5, l4 = lane >> 4, l15 = lane & 15;
  const unsigned ksrc = l4 * 256 + ((l15 ^ (4 * (wave & 3) + l4)) << 4);
  const unsigned vsrc = l4 * 256 + ((l15 ^ (l4 << 2)) << 4);
  const unsigned qvoff = r32 * 256 + h * 16;
  bf16x8 q[8];
  const bool isA = wave < 4;

#pragma unroll
  for (int i = 0; i < 10; ++i) { const unsigned o = wave * 1024 + i * 8192; dma16(ldsb + o, ksrc, rk, o); }
  if (isA) {
    const unsigned qsoff = wave * 8192; load_q(q, qvoff, rq, qsoff);
    asm volatile("s_waitcnt vmcnt(8)" ::: "memory");
    BARRIER();
    WAIT_Q(0);
  } else {
    asm volatile("s_waitcnt vmcnt(0)" ::: "memory");
    BARRIER();
#pragma unroll
    for (int i = 0; i < 20; ++i) { const unsigned o = (wave - 4) * 1024 + i * 4096; dma16(ldsb + VOFF + o, vsrc, rv, o); }
    const unsigned qsoff = wave * 8192; load_q(q, qvoff, rq, qsoff);
    asm volatile("s_waitcnt vmcnt(8)" ::: "memory");
    BARRIER();
    WAIT_Q(0);
  }

  const unsigned kaddr = r32 * 256 + ((h ^ (r32 & 15)) << 4);
  unsigned vaddr[4];
  {
    const unsigned qq = (lane >> 2) & 3, p = lane & 3, g1 = l4 & 1;
#pragma unroll
    for (int t = 0; t < 4; ++t) vaddr[t] = VOFF + (4 * h + qq) * 256 + 64 * (t ^ qq) + 32 * g1 + 16 * (p & 1) + 8 * (p >> 1);
  }

  f32x16 o[4]; float m, l;
  {
    f32x16 s[10];
    qk_phase<10>(s, q, lds, kaddr);
    if (isA) BARRIER();
    softmax_pv<10, 5>(s, o, m, l, lds, vaddr, [&]() {
      const unsigned qsoff = isA ? (8 + (wave >> 1)) * 8192 : 0x40000000u; load_q(q, qvoff, rq, qsoff); });
  }
  {
    const float sc = __builtin_amdgcn_rcpf(l);
    char* rowp = Og + (wave * 32 + r32) * 256 + h * 16;
#pragma unroll
    for (int t = 0; t < 4; ++t) store_tile(o[t], sc, rowp, t);
  }
  if (!isA) return;

  const unsigned kh = wave & 1;
  WAIT_Q(8);
  {
    f32x16 s[5];
    qk_phase<5>(s, q, lds, kaddr + kh * 40960);
#pragma unroll
    for (int t = 0; t < 4; ++t) vaddr[t] += kh * 40960;
    softmax_pv<5, -1>(s, o, m, l, lds, vaddr, []() {});
  }
  BARRIER();
  {
    LDS_AS char* mine = (LDS_AS char*)smem + wave * 9216 + lane * 16;
    const LDS_AS char* other = (const LDS_AS char*)smem + (wave ^ 1) * 9216 + lane * 16;
    const f32x16 s0 = kh ? o[0] : o[2], s1 = kh ? o[1] : o[3];
#pragma unroll
    for (int j = 0; j < 4; ++j) {
      *(LDS_AS f32x4*)(mine + j * 1024) = f32x4{s0[4 * j], s0[4 * j + 1], s0[4 * j + 2], s0[4 * j + 3]};
      *(LDS_AS f32x4*)(mine + (4 + j) * 1024) = f32x4{s1[4 * j], s1[4 * j + 1], s1[4 * j + 2], s1[4 * j + 3]};
    }
    *(LDS_AS f32x4*)(mine + 8 * 1024) = f32x4{m, l, 0.f, 0.f};
    asm volatile("s_waitcnt lgkmcnt(0)" ::: "memory");
    BARRIER();
    const f32x4 ml = *(const LDS_AS f32x4*)(other + 8 * 1024);
    const float mn = fmaxf(m, ml[0]);
    const float fa = __builtin_amdgcn_exp2f((m - mn) * CSC), fb = __builtin_amdgcn_exp2f((ml[0] - mn) * CSC);
    const float inv = __builtin_amdgcn_rcpf(l * fa + ml[1] * fb);
    const float ca = fa * inv, cb = fb * inv;
    f32x16 k0 = kh ? o[2] : o[0], k1 = kh ? o[3] : o[1];
#pragma unroll
    for (int j = 0; j < 4; ++j) {
      const f32x4 x0 = *(const LDS_AS f32x4*)(other + j * 1024), x1 = *(const LDS_AS f32x4*)(other + (4 + j) * 1024);
#pragma unroll
      for (int e = 0; e < 4; ++e) { k0[4 * j + e] = k0[4 * j + e] * ca + x0[e] * cb; k1[4 * j + e] = k1[4 * j + e] * ca + x1[e] * cb; }
    }
    char* rowp = Og + (256 + (wave >> 1) * 32 + r32) * 256 + h * 16 + kh * 128;
    store_tile(k0, 1.f, rowp, 0);
    store_tile(k1, 1.f, rowp, 1);
  }
}

extern "C" void kernel_launch(void* const* d_in, const int* in_sizes, int n_in,
                              void* d_out, int out_size, void* d_ws, size_t ws_size,
                              hipStream_t stream) {
  gqa_attn_kernel<<<dim3(NB * HQ), dim3(512), 0, stream>>>((const char*)d_in[0], (const char*)d_in[1], (const char*)d_in[2], (char*)d_out);
}
```

```cpp
#include <hip/hip_runtime.h>
#include <hip/hip_bf16.h>

typedef __bf16   bf16x8 __attribute__((ext_vector_type(8)));
typedef __bf16   bf16x2 __attribute__((ext_vector_type(2)));
typedef short    s16x4  __attribute__((ext_vector_type(4)));
typedef short    s16x8  __attribute__((ext_vector_type(8)));
typedef float    f32x16 __attribute__((ext_vector_type(16)));
typedef float    f32x4  __attribute__((ext_vector_type(4)));
typedef float    f32x2  __attribute__((ext_vector_type(2)));
typedef unsigned u32x4  __attribute__((ext_vector_type(4)));
#define DI __device__ __forceinline__
#define LDS_AS __attribute__((address_space(3)))

constexpr int NB = 8, HQ = 32, HKV = 8, SEQ = 320, HD = 128;
constexpr unsigned HEAD_BYTES = SEQ * HD * 2;
constexpr unsigned VOFF = HEAD_BYTES;
constexpr float CSC = 0.088388347648318440f * 1.4426950408889634f;

DI unsigned cvtpk(float lo, float hi) {
  f32x2 v = {lo, hi}; bf16x2 b = __builtin_convertvector(v, bf16x2); return __builtin_bit_cast(unsigned, b);
}
DI float xhalf_max(float x) {
  auto r = __builtin_amdgcn_permlane32_swap(__float_as_uint(x), __float_as_uint(x), false, false);
  return fmaxf(__uint_as_float(r[0]), __uint_as_float(r[1]));
}
DI float xhalf_sum(float x) {
  auto r = __builtin_amdgcn_permlane32_swap(__float_as_uint(x), __float_as_uint(x), false, false);
  return __uint_as_float(r[0]) + __uint_as_float(r[1]);
}
DI void dma16(unsigned m0v, unsigned voff, u32x4 rsrc, unsigned soff) {
  asm volatile("s_mov_b32 m0, %0\n\ts_nop 0\n\tbuffer_load_dwordx4 %1, %2, %3 offen lds"
               :: "s"(m0v), "v"(voff), "s"(rsrc), "s"(soff) : "memory");
}
#define QLOAD(i) asm volatile("buffer_load_dwordx4 %0, %1, %2, %3 offen offset:" #i : "=v"(q[(i) / 32]) : "v"(qvoff), "s"(rq), "s"(qsoff) : "memory")
DI void load_q(bf16x8 (&q)[8], unsigned qvoff, u32x4 rq, unsigned qsoff) {
  QLOAD(0); QLOAD(32); QLOAD(64); QLOAD(96); QLOAD(128); QLOAD(160); QLOAD(192); QLOAD(224);
}
#define WAIT_Q(N) asm volatile("s_waitcnt vmcnt(" #N ")" : "+v"(q[0]), "+v"(q[1]), "+v"(q[2]), "+v"(q[3]), "+v"(q[4]), "+v"(q[5]), "+v"(q[6]), "+v"(q[7]) :: "memory")
#define BARRIER() asm volatile("s_barrier" ::: "memory")
#define STAMP(i)
#define RSTAMP(i)
#define STAMP_FLUSH()
#define WS_PARAM
#define WS_ARG

template <int NKB>
DI void qk_phase(f32x16 (&s)[NKB], const bf16x8 (&q)[8], const LDS_AS char* lds, unsigned kaddr) {
#pragma unroll
  for (int kb = 0; kb < NKB; ++kb) {
    f32x16 acc = {};
#pragma unroll
    for (int st = 0; st < 8; ++st) {
      const bf16x8 a = *(const LDS_AS bf16x8*)(lds + ((kaddr ^ (unsigned)(st << 5)) + (unsigned)(kb * 8192)));
      acc = __builtin_amdgcn_mfma_f32_32x32x16_bf16(a, q[st], acc, 0, 0, 0);
    }
    s[kb] = acc;
  }
}

DI void exp_pack(const f32x16& s, float mc, float (&lp)[4], bf16x8 (&pb)[2]) {
  float p[16];
#pragma unroll
  for (int r = 0; r < 16; ++r) { p[r] = __builtin_amdgcn_exp2f(fmaf(s[r], CSC, mc)); lp[r & 3] += p[r]; }
#pragma unroll
  for (int ks = 0; ks < 2; ++ks) {
    u32x4 pw;
#pragma unroll
    for (int j = 0; j < 4; ++j) pw[j] = cvtpk(p[8 * ks + 2 * j], p[8 * ks + 2 * j + 1]);
    pb[ks] = __builtin_bit_cast(bf16x8, pw);
  }
  asm volatile("" : "+v"(lp[0]), "+v"(lp[1]), "+v"(lp[2]), "+v"(lp[3]));
}

template <int NKB, int HOOK_KB, class F>
DI void softmax_pv(f32x16 (&s)[NKB], f32x16 (&o)[4], float& m_out, float& l_out, const LDS_AS char* lds, const unsigned (&vaddr)[4], F hook) {
  float m = s[0][0];
#pragma unroll
  for (int kb = 0; kb < NKB; ++kb)
#pragma unroll
    for (int r = 0; r < 16; ++r) m = fmaxf(m, s[kb][r]);
  m = xhalf_max(m);
  const float mc = -m * CSC;
  float lp[4] = {0.f, 0.f, 0.f, 0.f};
#pragma unroll
  for (int t = 0; t < 4; ++t) o[t] = f32x16{};
  bf16x8 pb[2], pn[2];
  exp_pack(s[0], mc, lp, pb);
#pragma unroll
  for (int kb = 0; kb < NKB; ++kb) {
    __builtin_amdgcn_sched_barrier(0);
    if (kb == HOOK_KB) hook();
    if (kb + 1 < NKB) exp_pack(s[kb + 1], mc, lp, pn);
#pragma unroll
    for (int ks = 0; ks < 2; ++ks) {
#pragma unroll
      for (int t = 0; t < 4; ++t) {
        const unsigned off = (unsigned)((32 * kb + 16 * ks) * 256);
        const s16x4 lo = __builtin_amdgcn_ds_read_tr16_b64_v4i16((LDS_AS s16x4*)(lds + (vaddr[t] + off)));
        const s16x4 hi = __builtin_amdgcn_ds_read_tr16_b64_v4i16((LDS_AS s16x4*)(lds + (vaddr[t] + off + 2048u)));
        const s16x8 av = __builtin_shufflevector(lo, hi, 0, 1, 2, 3, 4, 5, 6, 7);
        o[t] = __builtin_amdgcn_mfma_f32_32x32x16_bf16(__builtin_bit_cast(bf16x8, av), pb[ks], o[t], 0, 0, 0);
      }
    }
    pb[0] = pn[0]; pb[1] = pn[1];
  }
  __builtin_amdgcn_sched_barrier(0);
  m_out = m;
  l_out = xhalf_sum((lp[0] + lp[1]) + (lp[2] + lp[3]));
}

DI void store_tile(const f32x16& o, float sc, char* rowp, int t) {
#pragma unroll
  for (int hf = 0; hf < 2; ++hf) {
    u32x4 w;
#pragma unroll
    for (int j = 0; j < 4; ++j) w[j] = cvtpk(o[8 * hf + 2 * j] * sc, o[8 * hf + 2 * j + 1] * sc);
    *(u32x4*)(rowp + 64 * t + 32 * hf) = w;
  }
}

__global__ __launch_bounds__(512, 2)
void gqa_attn_kernel(const char* __restrict__ Q, const char* __restrict__ K, const char* __restrict__ V, char* __restrict__ O WS_PARAM) {
  STAMP(0); RSTAMP(STAMPS - 2);
  __shared__ __attribute__((aligned(16))) char smem[2 * HEAD_BYTES];
  const LDS_AS char* lds = (const LDS_AS char*)smem;
  const unsigned ldsb = (unsigned)(size_t)(LDS_AS char*)smem;
  const unsigned lane = threadIdx.x & 63;
  const unsigned wave = __builtin_amdgcn_readfirstlane(threadIdx.x >> 6);
  const unsigned bid = blockIdx.x, xcd = bid & 7, idx = bid >> 3;
  const unsigned grp = xcd * 8 + (idx >> 2);
  const unsigned qh = grp * 4 + (idx & 3);
  const char* Kg = K + (size_t)grp * HEAD_BYTES;
  const char* Vg = V + (size_t)grp * HEAD_BYTES;
  const char* Qg = Q + (size_t)qh * HEAD_BYTES;
  char* Og = O + (size_t)qh * HEAD_BYTES;
  auto mk = [](const char* p) { u32x4 r; const unsigned long long a = (unsigned long long)p;
    r[0] = (unsigned)a; r[1] = (unsigned)(a >> 32) & 0xffffu; r[2] = HEAD_BYTES; r[3] = 0x00020000u; return r; };
  const u32x4 rk = mk(Kg), rv = mk(Vg), rq = mk(Qg);

  const unsigned r32 = lane & 31, h = lane >> 5, l4 = lane >> 4, l15 = lane & 15;
  const unsigned ksrc = l4 * 256 + ((l15 ^ (4 * (wave & 3) + l4)) << 4);
  const unsigned vsrc = l4 * 256 + ((l15 ^ (l4 << 2)) << 4);
  const unsigned qvoff = r32 * 256 + h * 16;
  bf16x8 q[8];
  const bool isA = wave < 4;

#pragma unroll
  for (int i = 0; i < 10; ++i) { const unsigned o = wave * 1024 + i * 8192; dma16(ldsb + o, ksrc, rk, o); }
  if (isA) {
    const unsigned qsoff = wave * 8192; load_q(q, qvoff, rq, qsoff);
    asm volatile("s_waitcnt vmcnt(8)" ::: "memory");
    BARRIER();
    STAMP(1);
    WAIT_Q(0);
  } else {
    asm volatile("s_waitcnt vmcnt(0)" ::: "memory");
    BARRIER();
    STAMP(1);
#pragma unroll
    for (int i = 0; i < 20; ++i) { const unsigned o = (wave - 4) * 1024 + i * 4096; dma16(ldsb + VOFF + o, vsrc, rv, o); }
    const unsigned qsoff = wave * 8192; load_q(q, qvoff, rq, qsoff);
    asm volatile("s_waitcnt vmcnt(8)" ::: "memory");
    BARRIER();
    WAIT_Q(0);
  }

  STAMP(2);
  const unsigned kaddr = r32 * 256 + ((h ^ (r32 & 15)) << 4);
  unsigned vaddr[4];
  {
    const unsigned qq = (lane >> 2) & 3, p = lane & 3, g1 = l4 & 1;
#pragma unroll
    for (int t = 0; t < 4; ++t) vaddr[t] = VOFF + (4 * h + qq) * 256 + 64 * (t ^ qq) + 32 * g1 + 16 * (p & 1) + 8 * (p >> 1);
  }

  f32x16 o[4]; float m, l;
  {
    f32x16 s[10];
    qk_phase<10>(s, q, lds, kaddr);
    STAMP(3);
    if (isA) BARRIER();
    STAMP(4);
    softmax_pv<10, 5>(s, o, m, l, lds, vaddr, [&]() {
      const unsigned qsoff = isA ? (8 + (wave >> 1)) * 8192 : 0x40000000u; load_q(q, qvoff, rq, qsoff); });
  }
  STAMP(5);
  {
    const float sc = __builtin_amdgcn_rcpf(l);
    char* rowp = Og + (wave * 32 + r32) * 256 + h * 16;
#pragma unroll
    for (int t = 0; t < 4; ++t) store_tile(o[t], sc, rowp, t);
  }
  STAMP(6);
  if (!isA) { STAMP_FLUSH(); return; }

  const unsigned kh = wave & 1;
  WAIT_Q(8);
  STAMP(7);
  {
    f32x16 s[5];
    qk_phase<5>(s, q, lds, kaddr + kh * 40960);
    STAMP(8);
#pragma unroll
    for (int t = 0; t < 4; ++t) vaddr[t] += kh * 40960;
    softmax_pv<5, -1>(s, o, m, l, lds, vaddr, []() {});
  }
  STAMP(9);
  BARRIER();
  {
    LDS_AS char* mine = (LDS_AS char*)smem + wave * 9216 + lane * 16;
    const LDS_AS char* other = (const LDS_AS char*)smem + (wave ^ 1) * 9216 + lane * 16;
    const f32x16 s0 = kh ? o[0] : o[2], s1 = kh ? o[1] : o[3];
#pragma unroll
    for (int j = 0; j < 4; ++j) {
      *(LDS_AS f32x4*)(mine + j * 1024) = f32x4{s0[4 * j], s0[4 * j + 1], s0[4 * j + 2], s0[4 * j + 3]};
      *(LDS_AS f32x4*)(mine + (4 + j) * 1024) = f32x4{s1[4 * j], s1[4 * j + 1], s1[4 * j + 2], s1[4 * j + 3]};
    }
    *(LDS_AS f32x4*)(mine + 8 * 1024) = f32x4{m, l, 0.f, 0.f};
    asm volatile("s_waitcnt lgkmcnt(0)" ::: "memory");
    BARRIER();
    const f32x4 ml = *(const LDS_AS f32x4*)(other + 8 * 1024);
    const float mn = fmaxf(m, ml[0]);
    const float fa = __builtin_amdgcn_exp2f((m - mn) * CSC), fb = __builtin_amdgcn_exp2f((ml[0] - mn) * CSC);
    const float inv = __builtin_amdgcn_rcpf(l * fa + ml[1] * fb);
    const float ca = fa * inv, cb = fb * inv;
    f32x16 k0 = kh ? o[2] : o[0], k1 = kh ? o[3] : o[1];
#pragma unroll
    for (int j = 0; j < 4; ++j) {
      const f32x4 x0 = *(const LDS_AS f32x4*)(other + j * 1024), x1 = *(const LDS_AS f32x4*)(other + (4 + j) * 1024);
#pragma unroll
      for (int e = 0; e < 4; ++e) { k0[4 * j + e] = k0[4 * j + e] * ca + x0[e] * cb; k1[4 * j + e] = k1[4 * j + e] * ca + x1[e] * cb; }
    }
    char* rowp = Og + (256 + (wave >> 1) * 32 + r32) * 256 + h * 16 + kh * 128;
    store_tile(k0, 1.f, rowp, 0);
    store_tile(k1, 1.f, rowp, 1);
  }
  STAMP(10);
  asm volatile("s_waitcnt vmcnt(0)" ::: "memory");
  STAMP(11);
  STAMP_FLUSH();
}

extern "C" void kernel_launch(void* const* d_in, const int* in_sizes, int n_in,
                              void* d_out, int out_size, void* d_ws, size_t ws_size,
                              hipStream_t stream) {
  gqa_attn_kernel<<<dim3(NB * HQ), dim3(512), 0, stream>>>((const char*)d_in[0], (const char*)d_in[1], (const char*)d_in[2], (char*)d_out WS_ARG);
}
```

```cpp
#include <hip/hip_runtime.h>
#include <hip/hip_bf16.h>

typedef __bf16   bf16x8 __attribute__((ext_vector_type(8)));
typedef __bf16   bf16x2 __attribute__((ext_vector_type(2)));
typedef short    s16x4  __attribute__((ext_vector_type(4)));
typedef short    s16x8  __attribute__((ext_vector_type(8)));
typedef float    f32x16 __attribute__((ext_vector_type(16)));
typedef float    f32x4  __attribute__((ext_vector_type(4)));
typedef float    f32x2  __attribute__((ext_vector_type(2)));
typedef unsigned u32x4  __attribute__((ext_vector_type(4)));
#define DI __device__ __forceinline__
#define LDS_AS __attribute__((address_space(3)))

constexpr int NB = 8, HQ = 32, HKV = 8, SEQ = 320, HD = 128;
constexpr unsigned HEAD_BYTES = SEQ * HD * 2;
constexpr unsigned VOFF = HEAD_BYTES;
constexpr float CSC = 0.088388347648318440f * 1.4426950408889634f;

DI unsigned cvtpk(float lo, float hi) {
  f32x2 v = {lo, hi}; bf16x2 b = __builtin_convertvector(v, bf16x2); return __builtin_bit_cast(unsigned, b);
}
DI float xhalf_max(float x) {
  auto r = __builtin_amdgcn_permlane32_swap(__float_as_uint(x), __float_as_uint(x), false, false);
  return fmaxf(__uint_as_float(r[0]), __uint_as_float(r[1]));
}
DI float xhalf_sum(float x) {
  auto r = __builtin_amdgcn_permlane32_swap(__float_as_uint(x), __float_as_uint(x), false, false);
  return __uint_as_float(r[0]) + __uint_as_float(r[1]);
}
DI void dma16(unsigned m0v, unsigned voff, u32x4 rsrc, unsigned soff) {
  asm volatile("s_mov_b32 m0, %0\n\ts_nop 0\n\tbuffer_load_dwordx4 %1, %2, %3 offen lds"
               :: "s"(m0v), "v"(voff), "s"(rsrc), "s"(soff) : "memory");
}
#define QLOAD(i) asm volatile("buffer_load_dwordx4 %0, %1, %2, %3 offen offset:" #i : "=v"(q[(i) / 32]) : "v"(qvoff), "s"(rq), "s"(qsoff) : "memory")
DI void load_q(bf16x8 (&q)[8], unsigned qvoff, u32x4 rq, unsigned qsoff) {
  QLOAD(0); QLOAD(32); QLOAD(64); QLOAD(96); QLOAD(128); QLOAD(160); QLOAD(192); QLOAD(224);
}
#define WAIT_Q(N) asm volatile("s_waitcnt vmcnt(" #N ")" : "+v"(q[0]), "+v"(q[1]), "+v"(q[2]), "+v"(q[3]), "+v"(q[4]), "+v"(q[5]), "+v"(q[6]), "+v"(q[7]) :: "memory")
#define SPIN_HOOK
#define BARRIER() asm volatile("s_barrier" ::: "memory")
#define STAMP(i)
#define RSTAMP(i)
#define STAMP_FLUSH()
#define WS_PARAM
#define WS_ARG

#define GAP() __builtin_amdgcn_sched_barrier(0)
DI float max3f(float a, float b, float c) { float r = fmaxf(fmaxf(a, b), c); asm volatile("" : "+v"(r)); return r; }
DI float fadd_s(float a, float b) { float r = a + b; asm volatile("" : "+v"(r)); return r; }

#define KDEPTH 6
#define VDEPTH 4
template <int NKB>
DI float qk_phase(f32x16 (&s)[NKB], const bf16x8 (&q)[8], const LDS_AS char* lds, const unsigned (&ka)[8]) {
  constexpr int N = NKB * 8;
  bf16x8 kf[KDEPTH];
#define KREAD(i) (*(const LDS_AS bf16x8*)(lds + (ka[(i) & 7] + (unsigned)(((i) >> 3) * 8192))))
#pragma unroll
  for (int i = 0; i < KDEPTH; ++i) kf[i] = KREAD(i);
  float m0 = -3.0e38f, m1 = -3.0e38f;
  GAP();
#pragma unroll
  for (int i = 0; i < N; ++i) {
    const int kb = i >> 3, st = i & 7;
    if (st == 0) s[kb] = __builtin_amdgcn_mfma_f32_32x32x16_bf16(kf[i % KDEPTH], q[0], f32x16{}, 0, 0, 0);
    else         s[kb] = __builtin_amdgcn_mfma_f32_32x32x16_bf16(kf[i % KDEPTH], q[st], s[kb], 0, 0, 0);
    if (i + KDEPTH < N) kf[i % KDEPTH] = KREAD(i + KDEPTH);
    if (kb > 0 && st >= 4) {
      const int r = 4 * (st - 4);
      m0 = max3f(m0, s[kb - 1][r], s[kb - 1][r + 1]);
      m1 = max3f(m1, s[kb - 1][r + 2], s[kb - 1][r + 3]);
    }
    GAP();
  }
#undef KREAD
#pragma unroll
  for (int r = 0; r < 16; r += 4) { m0 = fmaxf(fmaxf(m0, s[NKB - 1][r]), s[NKB - 1][r + 1]); m1 = fmaxf(fmaxf(m1, s[NKB - 1][r + 2]), s[NKB - 1][r + 3]); }
  return fmaxf(m0, m1);
}

DI unsigned exp_pair(float s0, float s1, float mc, float& l0, float& l1) {
  const float p0 = __builtin_amdgcn_exp2f(fmaf(s0, CSC, mc)), p1 = __builtin_amdgcn_exp2f(fmaf(s1, CSC, mc));
  l0 = fadd_s(l0, p0); l1 = fadd_s(l1, p1);
  return cvtpk(p0, p1);
}

template <int NKB, int HOOK_KB, class F, class G>
DI void softmax_pv(f32x16 (&s)[NKB], float m, f32x16 (&o)[4], float& l_out, const LDS_AS char* lds, const unsigned (&vaddr)[4], G pre, F hook) {
  constexpr int N = NKB * 8;
  const float mc = -m * CSC;
  float l0 = 0.f, l1 = 0.f;
  u32x4 pw[2][2];
#pragma unroll
  for (int j = 0; j < 8; ++j) pw[0][j >> 2][j & 3] = exp_pair(s[0][2 * j], s[0][2 * j + 1], mc, l0, l1);
  GAP();
  pre();
  s16x4 vlo[VDEPTH], vhi[VDEPTH];
#define VOFFS(i) ((unsigned)((((i) >> 2) * 16) * 256))
#define VREAD(i) do { vlo[(i) % VDEPTH] = __builtin_amdgcn_ds_read_tr16_b64_v4i16((LDS_AS s16x4*)(lds + (vaddr[(i) & 3] + VOFFS(i)))); \
                      vhi[(i) % VDEPTH] = __builtin_amdgcn_ds_read_tr16_b64_v4i16((LDS_AS s16x4*)(lds + (vaddr[(i) & 3] + VOFFS(i) + 2048u))); } while (0)
#pragma unroll
  for (int i = 0; i < VDEPTH; ++i) VREAD(i);
  GAP();
#pragma unroll
  for (int i = 0; i < N; ++i) {
    const int kb = i >> 3, ks = (i >> 2) & 1, t = i & 3, j = i & 7;
    if (j == 0 && kb == HOOK_KB) hook();
    const bf16x8 av = __builtin_bit_cast(bf16x8, __builtin_shufflevector(vlo[i % VDEPTH], vhi[i % VDEPTH], 0, 1, 2, 3, 4, 5, 6, 7));
    const bf16x8 pb = __builtin_bit_cast(bf16x8, pw[kb & 1][ks]);
    if (i < 4) o[t] = __builtin_amdgcn_mfma_f32_32x32x16_bf16(av, pb, f32x16{}, 0, 0, 0);
    else       o[t] = __builtin_amdgcn_mfma_f32_32x32x16_bf16(av, pb, o[t], 0, 0, 0);
    if (i + VDEPTH < N) VREAD(i + VDEPTH);
    if (kb + 1 < NKB) pw[(kb + 1) & 1][j >> 2][j & 3] = exp_pair(s[kb + 1][2 * j], s[kb + 1][2 * j + 1], mc, l0, l1);
    GAP();
  }
#undef VREAD
#undef VOFFS
  l_out = xhalf_sum(l0 + l1);
}

DI void store_tile(const f32x16& o, float sc, char* rowp, int t) {
#pragma unroll
  for (int hf = 0; hf < 2; ++hf) {
    u32x4 w;
#pragma unroll
    for (int j = 0; j < 4; ++j) w[j] = cvtpk(o[8 * hf + 2 * j] * sc, o[8 * hf + 2 * j + 1] * sc);
    *(u32x4*)(rowp + 64 * t + 32 * hf) = w;
  }
}

__global__ __launch_bounds__(512, 2)
void gqa_attn_kernel(const char* __restrict__ Q, const char* __restrict__ K, const char* __restrict__ V, char* __restrict__ O WS_PARAM) {
  STAMP(0); RSTAMP(STAMPS - 2);
  __shared__ __attribute__((aligned(16))) char smem[2 * HEAD_BYTES];
  const LDS_AS char* lds = (const LDS_AS char*)smem;
  const unsigned ldsb = (unsigned)(size_t)(LDS_AS char*)smem;
  const unsigned lane = threadIdx.x & 63;
  const unsigned wave = __builtin_amdgcn_readfirstlane(threadIdx.x >> 6);
  const unsigned bid = blockIdx.x, xcd = bid & 7, idx = bid >> 3;
  const unsigned grp = xcd * 8 + (idx >> 2);
  const unsigned qh = grp * 4 + (idx & 3);
  const char* Kg = K + (size_t)grp * HEAD_BYTES;
  const char* Vg = V + (size_t)grp * HEAD_BYTES;
  const char* Qg = Q + (size_t)qh * HEAD_BYTES;
  char* Og = O + (size_t)qh * HEAD_BYTES;
  auto mk = [](const char* p) { u32x4 r; const unsigned long long a = (unsigned long long)p;
    r[0] = (unsigned)a; r[1] = (unsigned)(a >> 32) & 0xffffu; r[2] = HEAD_BYTES; r[3] = 0x00020000u; return r; };
  const u32x4 rk = mk(Kg), rv = mk(Vg), rq = mk(Qg);

  const unsigned r32 = lane & 31, h = lane >> 5, l4 = lane >> 4, l15 = lane & 15;
  const unsigned ksrc = l4 * 256 + ((l15 ^ (4 * (wave & 3) + l4)) << 4);
  const unsigned vsrc = l4 * 256 + ((l15 ^ (l4 << 2)) << 4);
  const unsigned qvoff = r32 * 256 + h * 16;
  bf16x8 q[8];
  const bool isA = wave < 4;

#pragma unroll
  for (int i = 0; i < 10; ++i) { const unsigned o = wave * 1024 + i * 8192; dma16(ldsb + o, ksrc, rk, o); }
  { const unsigned qsoff = wave * 8192; load_q(q, qvoff, rq, qsoff); }
  if (isA) {
    asm volatile("s_waitcnt vmcnt(8)" ::: "memory");
  } else {
#pragma unroll
    for (int i = 0; i < 20; ++i) { const unsigned o = (wave - 4) * 1024 + i * 4096; dma16(ldsb + VOFF + o, vsrc, rv, o); }
    asm volatile("s_waitcnt vmcnt(28)" ::: "memory");
  }
  BARRIER();
  STAMP(1);
  asm volatile("s_cmp_lt_u32 %8, 4\n\ts_cbranch_scc1 1f\n\ts_waitcnt vmcnt(20)\n\ts_branch 2f\n1:\n\ts_waitcnt vmcnt(0)\n2:"
               : "+v"(q[0]), "+v"(q[1]), "+v"(q[2]), "+v"(q[3]), "+v"(q[4]), "+v"(q[5]), "+v"(q[6]), "+v"(q[7]) : "s"(wave) : "scc", "memory");
  SPIN_HOOK
  STAMP(2);
  unsigned ka[8];
#pragma unroll
  for (int st = 0; st < 8; ++st) ka[st] = r32 * 256 + (((2 * st + h) ^ (r32 & 15)) << 4);
  unsigned vaddr[4];
  {
    const unsigned qq = (lane >> 2) & 3, p = lane & 3, g1 = l4 & 1;
#pragma unroll
    for (int t = 0; t < 4; ++t) vaddr[t] = VOFF + (4 * h + qq) * 256 + 64 * (t ^ qq) + 32 * g1 + 16 * (p & 1) + 8 * (p >> 1);
  }

  f32x16 o[4]; float m, l;
  {
    f32x16 s[10];
    m = xhalf_max(qk_phase<10>(s, q, lds, ka));
    STAMP(3);
    softmax_pv<10, 5>(s, m, o, l, lds, vaddr,
      [&]() { asm volatile("s_waitcnt vmcnt(0)" ::: "memory"); BARRIER(); STAMP(4); },
      [&]() {
      const unsigned qsoff = isA ? (8 + (wave >> 1)) * 8192 : 0x40000000u; load_q(q, qvoff, rq, qsoff); });
  }
  STAMP(5);
  {
    const float sc = __builtin_amdgcn_rcpf(l);
    char* rowp = Og + (wave * 32 + r32) * 256 + h * 16;
#pragma unroll
    for (int t = 0; t < 4; ++t) store_tile(o[t], sc, rowp, t);
  }
  STAMP(6);
  if (!isA) { BARRIER(); BARRIER(); STAMP_FLUSH(); return; }

  const unsigned kh = wave & 1;
  WAIT_Q(8);
  STAMP(7);
  {
    f32x16 s[5];
#pragma unroll
    for (int st = 0; st < 8; ++st) ka[st] += kh * 40960;
    m = xhalf_max(qk_phase<5>(s, q, lds, ka));
    STAMP(8);
#pragma unroll
    for (int t = 0; t < 4; ++t) vaddr[t] += kh * 40960;
    softmax_pv<5, -1>(s, m, o, l, lds, vaddr, []() {}, []() {});
  }
  STAMP(9);
  BARRIER();
  {
    LDS_AS char* mine = (LDS_AS char*)smem + wave * 9216 + lane * 16;
    const LDS_AS char* other = (const LDS_AS char*)smem + (wave ^ 1) * 9216 + lane * 16;
    const f32x16 s0 = kh ? o[0] : o[2], s1 = kh ? o[1] : o[3];
#pragma unroll
    for (int j = 0; j < 4; ++j) {
      *(LDS_AS f32x4*)(mine + j * 1024) = f32x4{s0[4 * j], s0[4 * j + 1], s0[4 * j + 2], s0[4 * j + 3]};
      *(LDS_AS f32x4*)(mine + (4 + j) * 1024) = f32x4{s1[4 * j], s1[4 * j + 1], s1[4 * j + 2], s1[4 * j + 3]};
    }
    *(LDS_AS f32x4*)(mine + 8 * 1024) = f32x4{m, l, 0.f, 0.f};
    asm volatile("s_waitcnt lgkmcnt(0)" ::: "memory");
    BARRIER();
    const f32x4 ml = *(const LDS_AS f32x4*)(other + 8 * 1024);
    const float mn = fmaxf(m, ml[0]);
    const float fa = __builtin_amdgcn_exp2f((m - mn) * CSC), fb = __builtin_amdgcn_exp2f((ml[0] - mn) * CSC);
    const float inv = __builtin_amdgcn_rcpf(l * fa + ml[1] * fb);
    const float ca = fa * inv, cb = fb * inv;
    f32x16 k0 = kh ? o[2] : o[0], k1 = kh ? o[3] : o[1];
#pragma unroll
    for (int j = 0; j < 4; ++j) {
      const f32x4 x0 = *(const LDS_AS f32x4*)(other + j * 1024), x1 = *(const LDS_AS f32x4*)(other + (4 + j) * 1024);
#pragma unroll
      for (int e = 0; e < 4; ++e) { k0[4 * j + e] = k0[4 * j + e] * ca + x0[e] * cb; k1[4 * j + e] = k1[4 * j + e] * ca + x1[e] * cb; }
    }
    char* rowp = Og + (256 + (wave >> 1) * 32 + r32) * 256 + h * 16 + kh * 128;
    store_tile(k0, 1.f, rowp, 0);
    store_tile(k1, 1.f, rowp, 1);
  }
  STAMP(10);
  asm volatile("s_waitcnt vmcnt(0)" ::: "memory");
  STAMP(11);
  STAMP_FLUSH();
}

extern "C" void kernel_launch(void* const* d_in, const int* in_sizes, int n_in,
                              void* d_out, int out_size, void* d_ws, size_t ws_size,
                              hipStream_t stream) {
  gqa_attn_kernel<<<dim3(NB * HQ), dim3(512), 0, stream>>>((const char*)d_in[0], (const char*)d_in[1], (const char*)d_in[2], (char*)d_out WS_ARG);
}
```

```cpp
#define STORE_AUX 0
#include <hip/hip_runtime.h>
#include <hip/hip_bf16.h>

typedef __bf16   bf16x8 __attribute__((ext_vector_type(8)));
typedef __bf16   bf16x2 __attribute__((ext_vector_type(2)));
typedef short    s16x4  __attribute__((ext_vector_type(4)));
typedef short    s16x8  __attribute__((ext_vector_type(8)));
typedef float    f32x16 __attribute__((ext_vector_type(16)));
typedef float    f32x4  __attribute__((ext_vector_type(4)));
typedef float    f32x2  __attribute__((ext_vector_type(2)));
typedef unsigned u32x4  __attribute__((ext_vector_type(4)));
#define DI __device__ __forceinline__
#define LDS_AS __attribute__((address_space(3)))

constexpr int NB = 8, HQ = 32, HKV = 8, SEQ = 320, HD = 128;
constexpr unsigned HEAD_BYTES = SEQ * HD * 2;
constexpr unsigned VOFF = HEAD_BYTES;
constexpr float CSC = 0.088388347648318440f * 1.4426950408889634f;

DI unsigned cvtpk(float lo, float hi) {
  f32x2 v = {lo, hi}; bf16x2 b = __builtin_convertvector(v, bf16x2); return __builtin_bit_cast(unsigned, b);
}
DI float xhalf_max(float x) {
  auto r = __builtin_amdgcn_permlane32_swap(__float_as_uint(x), __float_as_uint(x), false, false);
  return fmaxf(__uint_as_float(r[0]), __uint_as_float(r[1]));
}
DI float xhalf_sum(float x) {
  auto r = __builtin_amdgcn_permlane32_swap(__float_as_uint(x), __float_as_uint(x), false, false);
  return __uint_as_float(r[0]) + __uint_as_float(r[1]);
}
DI void dma16(unsigned m0v, unsigned voff, u32x4 rsrc, unsigned soff) {
  asm volatile("s_mov_b32 m0, %0\n\ts_nop 0\n\tbuffer_load_dwordx4 %1, %2, %3 offen lds"
               :: "s"(m0v), "v"(voff), "s"(rsrc), "s"(soff) : "memory");
}
#define QLOAD(i) asm volatile("buffer_load_dwordx4 %0, %1, %2, %3 offen offset:" #i : "=v"(q[(i) / 32]) : "v"(qvoff), "s"(rq), "s"(qsoff) : "memory")
DI void load_q(bf16x8 (&q)[8], unsigned qvoff, u32x4 rq, unsigned qsoff) {
  QLOAD(0); QLOAD(32); QLOAD(64); QLOAD(96); QLOAD(128); QLOAD(160); QLOAD(192); QLOAD(224);
}
#define WAIT_Q(N) asm volatile("s_waitcnt vmcnt(" #N ")" : "+v"(q[0]), "+v"(q[1]), "+v"(q[2]), "+v"(q[3]), "+v"(q[4]), "+v"(q[5]), "+v"(q[6]), "+v"(q[7]) :: "memory")
#define SPIN_HOOK
#define BARRIER() asm volatile("s_barrier" ::: "memory")
#define STAMP(i)
#define RSTAMP(i)
#define STAMP_FLUSH()
#define WS_PARAM
#define WS_ARG

#define GAP() __builtin_amdgcn_sched_barrier(0)
DI float max3f(float a, float b, float c) { float r = fmaxf(fmaxf(a, b), c); asm volatile("" : "+v"(r)); return r; }
DI float fadd_s(float a, float b) { float r = a + b; asm volatile("" : "+v"(r)); return r; }

#define KDEPTH 6
#define VDEPTH 4
template <int NKB>
DI float qk_phase(f32x16 (&s)[NKB], const bf16x8 (&q)[8], const LDS_AS char* lds, const unsigned (&ka)[8]) {
  constexpr int N = NKB * 8;
  bf16x8 kf[KDEPTH];
#define KREAD(i) (*(const LDS_AS bf16x8*)(lds + (ka[(i) & 7] + (unsigned)(((i) >> 3) * 8192))))
#pragma unroll
  for (int i = 0; i < KDEPTH; ++i) kf[i] = KREAD(i);
  float m0 = -3.0e38f, m1 = -3.0e38f;
  GAP();
#pragma unroll
  for (int i = 0; i < N; ++i) {
    const int kb = i >> 3, st = i & 7;
    if (st == 0) s[kb] = __builtin_amdgcn_mfma_f32_32x32x16_bf16(kf[i % KDEPTH], q[0], f32x16{}, 0, 0, 0);
    else         s[kb] = __builtin_amdgcn_mfma_f32_32x32x16_bf16(kf[i % KDEPTH], q[st], s[kb], 0, 0, 0);
    if (i + KDEPTH < N) kf[i % KDEPTH] = KREAD(i + KDEPTH);
    if (kb > 0 && st >= 4) {
      const int r = 4 * (st - 4);
      m0 = max3f(m0, s[kb - 1][r], s[kb - 1][r + 1]);
      m1 = max3f(m1, s[kb - 1][r + 2], s[kb - 1][r + 3]);
    }
    GAP();
  }
#undef KREAD
#pragma unroll
  for (int r = 0; r < 16; r += 4) { m0 = fmaxf(fmaxf(m0, s[NKB - 1][r]), s[NKB - 1][r + 1]); m1 = fmaxf(fmaxf(m1, s[NKB - 1][r + 2]), s[NKB - 1][r + 3]); }
  return fmaxf(m0, m1);
}

DI unsigned exp_pair(float s0, float s1, float mc, float& l0, float& l1) {
  const float p0 = __builtin_amdgcn_exp2f(fmaf(s0, CSC, mc)), p1 = __builtin_amdgcn_exp2f(fmaf(s1, CSC, mc));
  l0 = fadd_s(l0, p0); l1 = fadd_s(l1, p1);
  return cvtpk(p0, p1);
}

template <int NKB, int HOOK_KB, class F, class G>
DI void softmax_pv(f32x16 (&s)[NKB], float m, f32x16 (&o)[4], float& l_out, const LDS_AS char* lds, const unsigned (&vaddr)[4], G pre, F hook) {
  constexpr int N = NKB * 8;
  const float mc = -m * CSC;
  float l0 = 0.f, l1 = 0.f;
  u32x4 pw[2][2];
#pragma unroll
  for (int j = 0; j < 8; ++j) pw[0][j >> 2][j & 3] = exp_pair(s[0][2 * j], s[0][2 * j + 1], mc, l0, l1);
  GAP();
  pre();
  s16x4 vlo[VDEPTH], vhi[VDEPTH];
#define VOFFS(i) ((unsigned)((((i) >> 2) * 16) * 256))
#define VREAD(i) do { vlo[(i) % VDEPTH] = __builtin_amdgcn_ds_read_tr16_b64_v4i16((LDS_AS s16x4*)(lds + (vaddr[(i) & 3] + VOFFS(i)))); \
                      vhi[(i) % VDEPTH] = __builtin_amdgcn_ds_read_tr16_b64_v4i16((LDS_AS s16x4*)(lds + (vaddr[(i) & 3] + VOFFS(i) + 2048u))); } while (0)
#pragma unroll
  for (int i = 0; i < VDEPTH; ++i) VREAD(i);
  GAP();
#pragma unroll
  for (int i = 0; i < N; ++i) {
    const int kb = i >> 3, ks = (i >> 2) & 1, t = i & 3, j = i & 7;
    if (j == 0 && kb == HOOK_KB) hook();
    const bf16x8 av = __builtin_bit_cast(bf16x8, __builtin_shufflevector(vlo[i % VDEPTH], vhi[i % VDEPTH], 0, 1, 2, 3, 4, 5, 6, 7));
    const bf16x8 pb = __builtin_bit_cast(bf16x8, pw[kb & 1][ks]);
    if (i < 4) o[t] = __builtin_amdgcn_mfma_f32_32x32x16_bf16(av, pb, f32x16{}, 0, 0, 0);
    else       o[t] = __builtin_amdgcn_mfma_f32_32x32x16_bf16(av, pb, o[t], 0, 0, 0);
    if (i + VDEPTH < N) VREAD(i + VDEPTH);
    if (kb + 1 < NKB) pw[(kb + 1) & 1][j >> 2][j & 3] = exp_pair(s[kb + 1][2 * j], s[kb + 1][2 * j + 1], mc, l0, l1);
    GAP();
  }
#undef VREAD
#undef VOFFS
  l_out = xhalf_sum(l0 + l1);
}

DI void store_tile(const f32x16& o, float sc, __amdgpu_buffer_rsrc_t ro, unsigned rowp, int t) {
#pragma unroll
  for (int hf = 0; hf < 2; ++hf) {
    u32x4 w;
#pragma unroll
    for (int j = 0; j < 4; ++j) w[j] = cvtpk(o[8 * hf + 2 * j] * sc, o[8 * hf + 2 * j + 1] * sc);
    __builtin_amdgcn_raw_buffer_store_b128(w, ro, rowp + 64 * t + 32 * hf, 0, STORE_AUX);
  }
}

__global__ __launch_bounds__(512, 2)
void gqa_attn_kernel(const char* __restrict__ Q, const char* __restrict__ K, const char* __restrict__ V, char* __restrict__ O WS_PARAM) {
  STAMP(0); RSTAMP(STAMPS - 2);
  __shared__ __attribute__((aligned(16))) char smem[2 * HEAD_BYTES];
  const LDS_AS char* lds = (const LDS_AS char*)smem;
  const unsigned ldsb = (unsigned)(size_t)(LDS_AS char*)smem;
  const unsigned lane = threadIdx.x & 63;
  const unsigned wave = __builtin_amdgcn_readfirstlane(threadIdx.x >> 6);
  const unsigned bid = blockIdx.x, xcd = bid & 7, idx = bid >> 3;
  const unsigned grp = xcd * 8 + (idx >> 2);
  const unsigned qh = grp * 4 + (idx & 3);
  const char* Kg = K + (size_t)grp * HEAD_BYTES;
  const char* Vg = V + (size_t)grp * HEAD_BYTES;
  const char* Qg = Q + (size_t)qh * HEAD_BYTES;
  const __amdgpu_buffer_rsrc_t ro = __builtin_amdgcn_make_buffer_rsrc(O + (size_t)qh * HEAD_BYTES, 0, HEAD_BYTES, 0x00020000);
  auto mk = [](const char* p) { u32x4 r; const unsigned long long a = (unsigned long long)p;
    r[0] = (unsigned)a; r[1] = (unsigned)(a >> 32) & 0xffffu; r[2] = HEAD_BYTES; r[3] = 0x00020000u; return r; };
  const u32x4 rk = mk(Kg), rv = mk(Vg), rq = mk(Qg);

  const unsigned r32 = lane & 31, h = lane >> 5, l4 = lane >> 4, l15 = lane & 15;
  const unsigned ksrc = l4 * 256 + ((l15 ^ (4 * (wave & 3) + l4)) << 4);
  const unsigned vsrc = l4 * 256 + ((l15 ^ (l4 << 2)) << 4);
  const unsigned qvoff = r32 * 256 + h * 16;
  bf16x8 q[8];
  const bool isA = wave < 4;

#pragma unroll
  for (int i = 0; i < 10; ++i) { const unsigned o = wave * 1024 + i * 8192; dma16(ldsb + o, ksrc, rk, o); }
  { const unsigned qsoff = wave * 8192; load_q(q, qvoff, rq, qsoff); }
  if (isA) {
    asm volatile("s_waitcnt vmcnt(8)" ::: "memory");
  } else {
#pragma unroll
    for (int i = 0; i < 20; ++i) { const unsigned o = (wave - 4) * 1024 + i * 4096; dma16(ldsb + VOFF + o, vsrc, rv, o); }
    asm volatile("s_waitcnt vmcnt(28)" ::: "memory");
  }
  BARRIER();
  STAMP(1);
  asm volatile("s_cmp_lt_u32 %8, 4\n\ts_cbranch_scc1 1f\n\ts_waitcnt vmcnt(20)\n\ts_branch 2f\n1:\n\ts_waitcnt vmcnt(0)\n2:"
               : "+v"(q[0]), "+v"(q[1]), "+v"(q[2]), "+v"(q[3]), "+v"(q[4]), "+v"(q[5]), "+v"(q[6]), "+v"(q[7]) : "s"(wave) : "scc", "memory");
  SPIN_HOOK
  STAMP(2);
  unsigned ka[8];
#pragma unroll
  for (int st = 0; st < 8; ++st) ka[st] = r32 * 256 + (((2 * st + h) ^ (r32 & 15)) << 4);
  unsigned vaddr[4];
  {
    const unsigned qq = (lane >> 2) & 3, p = lane & 3, g1 = l4 & 1;
#pragma unroll
    for (int t = 0; t < 4; ++t) vaddr[t] = VOFF + (4 * h + qq) * 256 + 64 * (t ^ qq) + 32 * g1 + 16 * (p & 1) + 8 * (p >> 1);
  }

  f32x16 o[4]; float m, l;
  {
    f32x16 s[10];
    m = xhalf_max(qk_phase<10>(s, q, lds, ka));
    STAMP(3);
    softmax_pv<10, 5>(s, m, o, l, lds, vaddr,
      [&]() { asm volatile("s_waitcnt vmcnt(0)" ::: "memory"); BARRIER(); STAMP(4); },
      [&]() {
      const unsigned qsoff = isA ? (8 + (wave >> 1)) * 8192 : 0x40000000u; load_q(q, qvoff, rq, qsoff); });
  }
  STAMP(5);
  {
    const float sc = __builtin_amdgcn_rcpf(l);
    const unsigned rowp = wave * 8192 + qvoff;
#pragma unroll
    for (int t = 0; t < 4; ++t) store_tile(o[t], sc, ro, rowp, t);
  }
  STAMP(6);
  if (!isA) { BARRIER(); BARRIER(); STAMP_FLUSH(); return; }

  const unsigned kh = wave & 1;
  WAIT_Q(8);
  STAMP(7);
  {
    f32x16 s[5];
#pragma unroll
    for (int st = 0; st < 8; ++st) ka[st] += kh * 40960;
    m = xhalf_max(qk_phase<5>(s, q, lds, ka));
    STAMP(8);
#pragma unroll
    for (int t = 0; t < 4; ++t) vaddr[t] += kh * 40960;
    softmax_pv<5, -1>(s, m, o, l, lds, vaddr, []() {}, []() {});
  }
  STAMP(9);
  BARRIER();
  {
    LDS_AS char* mine = (LDS_AS char*)smem + wave * 9216 + lane * 16;
    const LDS_AS char* other = (const LDS_AS char*)smem + (wave ^ 1) * 9216 + lane * 16;
    const f32x16 s0 = kh ? o[0] : o[2], s1 = kh ? o[1] : o[3];
#pragma unroll
    for (int j = 0; j < 4; ++j) {
      *(LDS_AS f32x4*)(mine + j * 1024) = f32x4{s0[4 * j], s0[4 * j + 1], s0[4 * j + 2], s0[4 * j + 3]};
      *(LDS_AS f32x4*)(mine + (4 + j) * 1024) = f32x4{s1[4 * j], s1[4 * j + 1], s1[4 * j + 2], s1[4 * j + 3]};
    }
    *(LDS_AS f32x4*)(mine + 8 * 1024) = f32x4{m, l, 0.f, 0.f};
    asm volatile("s_waitcnt lgkmcnt(0)" ::: "memory");
    BARRIER();
    const f32x4 ml = *(const LDS_AS f32x4*)(other + 8 * 1024);
    const float mn = fmaxf(m, ml[0]);
    const float fa = __builtin_amdgcn_exp2f((m - mn) * CSC), fb = __builtin_amdgcn_exp2f((ml[0] - mn) * CSC);
    const float inv = __builtin_amdgcn_rcpf(l * fa + ml[1] * fb);
    const float ca = fa * inv, cb = fb * inv;
    f32x16 k0 = kh ? o[2] : o[0], k1 = kh ? o[3] : o[1];
#pragma unroll
    for (int j = 0; j < 4; ++j) {
      const f32x4 x0 = *(const LDS_AS f32x4*)(other + j * 1024), x1 = *(const LDS_AS f32x4*)(other + (4 + j) * 1024);
#pragma unroll
      for (int e = 0; e < 4; ++e) { k0[4 * j + e] = k0[4 * j + e] * ca + x0[e] * cb; k1[4 * j + e] = k1[4 * j + e] * ca + x1[e] * cb; }
    }
    const unsigned rowp = (8 + (wave >> 1)) * 8192 + kh * 128 + qvoff;
    store_tile(k0, 1.f, ro, rowp, 0);
    store_tile(k1, 1.f, ro, rowp, 1);
  }
  STAMP(10);
  asm volatile("s_waitcnt vmcnt(0)" ::: "memory");
  STAMP(11);
  STAMP_FLUSH();
}

extern "C" void kernel_launch(void* const* d_in, const int* in_sizes, int n_in,
                              void* d_out, int out_size, void* d_ws, size_t ws_size,
                              hipStream_t stream) {
  gqa_attn_kernel<<<dim3(NB * HQ), dim3(512), 0, stream>>>((const char*)d_in[0], (const char*)d_in[1], (const char*)d_in[2], (char*)d_out WS_ARG);
}
```

```cpp
#define SPIN_T0 1
#define SPINX(x) while (__builtin_amdgcn_s_memrealtime() - spin_t0 < (x)) __builtin_amdgcn_s_sleep(1);
#include <hip/hip_runtime.h>
#include <hip/hip_bf16.h>

typedef __bf16   bf16x8 __attribute__((ext_vector_type(8)));
typedef __bf16   bf16x2 __attribute__((ext_vector_type(2)));
typedef short    s16x4  __attribute__((ext_vector_type(4)));
typedef short    s16x8  __attribute__((ext_vector_type(8)));
typedef float    f32x16 __attribute__((ext_vector_type(16)));
typedef float    f32x4  __attribute__((ext_vector_type(4)));
typedef float    f32x2  __attribute__((ext_vector_type(2)));
typedef unsigned u32x4  __attribute__((ext_vector_type(4)));
#define DI __device__ __forceinline__
#define LDS_AS __attribute__((address_space(3)))

constexpr int NB = 8, HQ = 32, HKV = 8, SEQ = 320, HD = 128;
constexpr unsigned HEAD_BYTES = SEQ * HD * 2;
constexpr unsigned VOFF = HEAD_BYTES;
constexpr float CSC = 0.088388347648318440f * 1.4426950408889634f;

DI unsigned cvtpk(float lo, float hi) {
  f32x2 v = {lo, hi}; bf16x2 b = __builtin_convertvector(v, bf16x2); return __builtin_bit_cast(unsigned, b);
}
DI float xhalf_max(float x) {
  auto r = __builtin_amdgcn_permlane32_swap(__float_as_uint(x), __float_as_uint(x), false, false);
  return fmaxf(__uint_as_float(r[0]), __uint_as_float(r[1]));
}
DI float xhalf_sum(float x) {
  auto r = __builtin_amdgcn_permlane32_swap(__float_as_uint(x), __float_as_uint(x), false, false);
  return __uint_as_float(r[0]) + __uint_as_float(r[1]);
}
DI void dma16(unsigned m0v, unsigned voff, u32x4 rsrc, unsigned soff) {
  asm volatile("s_mov_b32 m0, %0\n\ts_nop 0\n\tbuffer_load_dwordx4 %1, %2, %3 offen lds"
               :: "s"(m0v), "v"(voff), "s"(rsrc), "s"(soff) : "memory");
}
#define QLOAD(i) asm volatile("buffer_load_dwordx4 %0, %1, %2, %3 offen offset:" #i : "=v"(q[(i) / 32]) : "v"(qvoff), "s"(rq), "s"(qsoff) : "memory")
DI void load_q(bf16x8 (&q)[8], unsigned qvoff, u32x4 rq, unsigned qsoff) {
  QLOAD(0); QLOAD(32); QLOAD(64); QLOAD(96); QLOAD(128); QLOAD(160); QLOAD(192); QLOAD(224);
}
#define WAIT_Q(N) asm volatile("s_waitcnt vmcnt(" #N ")" : "+v"(q[0]), "+v"(q[1]), "+v"(q[2]), "+v"(q[3]), "+v"(q[4]), "+v"(q[5]), "+v"(q[6]), "+v"(q[7]) :: "memory")
#define SPIN_HOOK
#define BARRIER() asm volatile("s_barrier" ::: "memory")
#define STAMP(i)
#define RSTAMP(i)
#define STAMP_FLUSH()
#define WS_PARAM
#define WS_ARG

#define GAP() __builtin_amdgcn_sched_barrier(0)
DI float max3f(float a, float b, float c) { float r = fmaxf(fmaxf(a, b), c); asm volatile("" : "+v"(r)); return r; }
DI float fadd_s(float a, float b) { float r = a + b; asm volatile("" : "+v"(r)); return r; }

#define KDEPTH 6
#define VDEPTH 4
template <int NKB>
DI float qk_phase(f32x16 (&s)[NKB], const bf16x8 (&q)[8], const LDS_AS char* lds, const unsigned (&ka)[8]) {
  constexpr int N = NKB * 8;
  bf16x8 kf[KDEPTH];
#define KREAD(i) (*(const LDS_AS bf16x8*)(lds + (ka[(i) & 7] + (unsigned)(((i) >> 3) * 8192))))
#pragma unroll
  for (int i = 0; i < KDEPTH; ++i) kf[i] = KREAD(i);
  float m0 = -3.0e38f, m1 = -3.0e38f;
  GAP();
#pragma unroll
  for (int i = 0; i < N; ++i) {
    const int kb = i >> 3, st = i & 7;
    if (st == 0) s[kb] = __builtin_amdgcn_mfma_f32_32x32x16_bf16(kf[i % KDEPTH], q[0], f32x16{}, 0, 0, 0);
    else         s[kb] = __builtin_amdgcn_mfma_f32_32x32x16_bf16(kf[i % KDEPTH], q[st], s[kb], 0, 0, 0);
    if (i + KDEPTH < N) kf[i % KDEPTH] = KREAD(i + KDEPTH);
    if (kb > 0 && st >= 4) {
      const int r = 4 * (st - 4);
      m0 = max3f(m0, s[kb - 1][r], s[kb - 1][r + 1]);
      m1 = max3f(m1, s[kb - 1][r + 2], s[kb - 1][r + 3]);
    }
    GAP();
  }
#undef KREAD
#pragma unroll
  for (int r = 0; r < 16; r += 4) { m0 = fmaxf(fmaxf(m0, s[NKB - 1][r]), s[NKB - 1][r + 1]); m1 = fmaxf(fmaxf(m1, s[NKB - 1][r + 2]), s[NKB - 1][r + 3]); }
  return fmaxf(m0, m1);
}

DI unsigned exp_pair(float s0, float s1, float mc, float& l0, float& l1) {
  const float p0 = __builtin_amdgcn_exp2f(fmaf(s0, CSC, mc)), p1 = __builtin_amdgcn_exp2f(fmaf(s1, CSC, mc));
  l0 = fadd_s(l0, p0); l1 = fadd_s(l1, p1);
  return cvtpk(p0, p1);
}

template <int NKB, int HOOK_KB, class F, class G>
DI void softmax_pv(f32x16 (&s)[NKB], float m, f32x16 (&o)[4], float& l_out, const LDS_AS char* lds, const unsigned (&vaddr)[4], G pre, F hook) {
  constexpr int N = NKB * 8;
  const float mc = -m * CSC;
  float l0 = 0.f, l1 = 0.f;
  u32x4 pw[2][2];
#pragma unroll
  for (int j = 0; j < 8; ++j) pw[0][j >> 2][j & 3] = exp_pair(s[0][2 * j], s[0][2 * j + 1], mc, l0, l1);
  GAP();
  pre();
  s16x4 vlo[VDEPTH], vhi[VDEPTH];
#define VOFFS(i) ((unsigned)((((i) >> 2) * 16) * 256))
#define VREAD(i) do { vlo[(i) % VDEPTH] = __builtin_amdgcn_ds_read_tr16_b64_v4i16((LDS_AS s16x4*)(lds + (vaddr[(i) & 3] + VOFFS(i)))); \
                      vhi[(i) % VDEPTH] = __builtin_amdgcn_ds_read_tr16_b64_v4i16((LDS_AS s16x4*)(lds + (vaddr[(i) & 3] + VOFFS(i) + 2048u))); } while (0)
#pragma unroll
  for (int i = 0; i < VDEPTH; ++i) VREAD(i);
  GAP();
#pragma unroll
  for (int i = 0; i < N; ++i) {
    const int kb = i >> 3, ks = (i >> 2) & 1, t = i & 3, j = i & 7;
    if (j == 0 && kb == HOOK_KB) hook();
    const bf16x8 av = __builtin_bit_cast(bf16x8, __builtin_shufflevector(vlo[i % VDEPTH], vhi[i % VDEPTH], 0, 1, 2, 3, 4, 5, 6, 7));
    const bf16x8 pb = __builtin_bit_cast(bf16x8, pw[kb & 1][ks]);
    if (i < 4) o[t] = __builtin_amdgcn_mfma_f32_32x32x16_bf16(av, pb, f32x16{}, 0, 0, 0);
    else       o[t] = __builtin_amdgcn_mfma_f32_32x32x16_bf16(av, pb, o[t], 0, 0, 0);
    if (i + VDEPTH < N) VREAD(i + VDEPTH);
    if (kb + 1 < NKB) pw[(kb + 1) & 1][j >> 2][j & 3] = exp_pair(s[kb + 1][2 * j], s[kb + 1][2 * j + 1], mc, l0, l1);
    GAP();
  }
#undef VREAD
#undef VOFFS
  l_out = xhalf_sum(l0 + l1);
}

#define STORE_AUX 0
DI void store_tile(const f32x16& o, float sc, __amdgpu_buffer_rsrc_t ro, unsigned rowp, int t) {
#pragma unroll
  for (int hf = 0; hf < 2; ++hf) {
    u32x4 w;
#pragma unroll
    for (int j = 0; j < 4; ++j) w[j] = cvtpk(o[8 * hf + 2 * j] * sc, o[8 * hf + 2 * j + 1] * sc);
    __builtin_amdgcn_raw_buffer_store_b128(w, ro, rowp + 64 * t + 32 * hf, 0, STORE_AUX);
  }
}

__global__ __launch_bounds__(512, 2)
void gqa_attn_kernel(const char* __restrict__ Q, const char* __restrict__ K, const char* __restrict__ V, char* __restrict__ O WS_PARAM) {
  STAMP(0); RSTAMP(STAMPS - 2);
  const unsigned long long spin_t0 = __builtin_amdgcn_s_memrealtime();
  __shared__ __attribute__((aligned(16))) char smem[2 * HEAD_BYTES];
  const LDS_AS char* lds = (const LDS_AS char*)smem;
  const unsigned ldsb = (unsigned)(size_t)(LDS_AS char*)smem;
  const unsigned lane = threadIdx.x & 63;
  const unsigned wave = __builtin_amdgcn_readfirstlane(threadIdx.x >> 6);
  const unsigned bid = blockIdx.x, xcd = bid & 7, idx = bid >> 3;
  const unsigned grp = xcd * 8 + (idx >> 2);
  const unsigned qh = grp * 4 + (idx & 3);
  const char* Kg = K + (size_t)grp * HEAD_BYTES;
  const char* Vg = V + (size_t)grp * HEAD_BYTES;
  const char* Qg = Q + (size_t)qh * HEAD_BYTES;
  const __amdgpu_buffer_rsrc_t ro = __builtin_amdgcn_make_buffer_rsrc(O + (size_t)qh * HEAD_BYTES, 0, HEAD_BYTES, 0x00020000);
  auto mk = [](const char* p) { u32x4 r; const unsigned long long a = (unsigned long long)p;
    r[0] = (unsigned)a; r[1] = (unsigned)(a >> 32) & 0xffffu; r[2] = HEAD_BYTES; r[3] = 0x00020000u; return r; };
  const u32x4 rk = mk(Kg), rv = mk(Vg), rq = mk(Qg);

  const unsigned r32 = lane & 31, h = lane >> 5, l4 = lane >> 4, l15 = lane & 15;
  const unsigned ksrc = l4 * 256 + ((l15 ^ (4 * (wave & 3) + l4)) << 4);
  const unsigned vsrc = l4 * 256 + ((l15 ^ (l4 << 2)) << 4);
  const unsigned qvoff = r32 * 256 + h * 16;
  bf16x8 q[8];
  const bool isA = wave < 4;

#pragma unroll
  for (int i = 0; i < 10; ++i) { const unsigned o = wave * 1024 + i * 8192; dma16(ldsb + o, ksrc, rk, o); }
  { const unsigned qsoff = wave * 8192; load_q(q, qvoff, rq, qsoff); }
  if (isA) {
    asm volatile("s_waitcnt vmcnt(8)" ::: "memory");
  } else {
#pragma unroll
    for (int i = 0; i < 20; ++i) { const unsigned o = (wave - 4) * 1024 + i * 4096; dma16(ldsb + VOFF + o, vsrc, rv, o); }
    asm volatile("s_waitcnt vmcnt(28)" ::: "memory");
  }
  BARRIER();
  STAMP(1);
  asm volatile("s_cmp_lt_u32 %8, 4\n\ts_cbranch_scc1 1f\n\ts_waitcnt vmcnt(20)\n\ts_branch 2f\n1:\n\ts_waitcnt vmcnt(0)\n2:"
               : "+v"(q[0]), "+v"(q[1]), "+v"(q[2]), "+v"(q[3]), "+v"(q[4]), "+v"(q[5]), "+v"(q[6]), "+v"(q[7]) : "s"(wave) : "scc", "memory");
  SPIN_HOOK
  STAMP(2);
  unsigned ka[8];
#pragma unroll
  for (int st = 0; st < 8; ++st) ka[st] = r32 * 256 + (((2 * st + h) ^ (r32 & 15)) << 4);
  unsigned vaddr[4];
  {
    const unsigned qq = (lane >> 2) & 3, p = lane & 3, g1 = l4 & 1;
#pragma unroll
    for (int t = 0; t < 4; ++t) vaddr[t] = VOFF + (4 * h + qq) * 256 + 64 * (t ^ qq) + 32 * g1 + 16 * (p & 1) + 8 * (p >> 1);
  }

  f32x16 o[4]; float m, l;
  {
    f32x16 s[10];
    m = xhalf_max(qk_phase<10>(s, q, lds, ka));
    STAMP(3);
    softmax_pv<10, 5>(s, m, o, l, lds, vaddr,
      [&]() { asm volatile("s_waitcnt vmcnt(0)" ::: "memory"); BARRIER(); STAMP(4); },
      [&]() {
      const unsigned qsoff = isA ? (8 + (wave >> 1)) * 8192 : 0x40000000u; load_q(q, qvoff, rq, qsoff); });
  }
  STAMP(5);
  {
    const float sc = __builtin_amdgcn_rcpf(l);
    const unsigned rowp = wave * 8192 + qvoff;
#pragma unroll
    for (int t = 0; t < 4; ++t) store_tile(o[t], sc, ro, rowp, t);
  }
  STAMP(6);
  if (!isA) { BARRIER(); BARRIER(); STAMP_FLUSH(); return; }

  const unsigned kh = wave & 1;
  WAIT_Q(8);
  STAMP(7);
  {
    f32x16 s[5];
#pragma unroll
    for (int st = 0; st < 8; ++st) ka[st] += kh * 40960;
    m = xhalf_max(qk_phase<5>(s, q, lds, ka));
    STAMP(8);
#pragma unroll
    for (int t = 0; t < 4; ++t) vaddr[t] += kh * 40960;
    softmax_pv<5, -1>(s, m, o, l, lds, vaddr, []() {}, []() {});
  }
  STAMP(9);
  BARRIER();
  {
    LDS_AS char* mine = (LDS_AS char*)smem + wave * 9216 + lane * 16;
    const LDS_AS char* other = (const LDS_AS char*)smem + (wave ^ 1) * 9216 + lane * 16;
    const f32x16 s0 = kh ? o[0] : o[2], s1 = kh ? o[1] : o[3];
#pragma unroll
    for (int j = 0; j < 4; ++j) {
      *(LDS_AS f32x4*)(mine + j * 1024) = f32x4{s0[4 * j], s0[4 * j + 1], s0[4 * j + 2], s0[4 * j + 3]};
      *(LDS_AS f32x4*)(mine + (4 + j) * 1024) = f32x4{s1[4 * j], s1[4 * j + 1], s1[4 * j + 2], s1[4 * j + 3]};
    }
    *(LDS_AS f32x4*)(mine + 8 * 1024) = f32x4{m, l, 0.f, 0.f};
    asm volatile("s_waitcnt lgkmcnt(0)" ::: "memory");
    BARRIER();
    const f32x4 ml = *(const LDS_AS f32x4*)(other + 8 * 1024);
    const float mn = fmaxf(m, ml[0]);
    const float fa = __builtin_amdgcn_exp2f((m - mn) * CSC), fb = __builtin_amdgcn_exp2f((ml[0] - mn) * CSC);
    const float inv = __builtin_amdgcn_rcpf(l * fa + ml[1] * fb);
    const float ca = fa * inv, cb = fb * inv;
    f32x16 k0 = kh ? o[2] : o[0], k1 = kh ? o[3] : o[1];
#pragma unroll
    for (int j = 0; j < 4; ++j) {
      const f32x4 x0 = *(const LDS_AS f32x4*)(other + j * 1024), x1 = *(const LDS_AS f32x4*)(other + (4 + j) * 1024);
#pragma unroll
      for (int e = 0; e < 4; ++e) { k0[4 * j + e] = k0[4 * j + e] * ca + x0[e] * cb; k1[4 * j + e] = k1[4 * j + e] * ca + x1[e] * cb; }
    }
    const unsigned rowp = (8 + (wave >> 1)) * 8192 + kh * 128 + qvoff;
    store_tile(k0, 1.f, ro, rowp, 0);
    store_tile(k1, 1.f, ro, rowp, 1);
  }
  SPINX(2500ull)
  if (wave == 0) { __builtin_amdgcn_fence(__ATOMIC_RELEASE, "agent"); asm volatile("s_waitcnt vmcnt(0)" ::: "memory"); }
  SPINX(4000ull)
  asm volatile("s_waitcnt vmcnt(0)" ::: "memory");
  STAMP(11);
  STAMP_FLUSH();
}

extern "C" void kernel_launch(void* const* d_in, const int* in_sizes, int n_in,
                              void* d_out, int out_size, void* d_ws, size_t ws_size,
                              hipStream_t stream) {
  gqa_attn_kernel<<<dim3(NB * HQ), dim3(512), 0, stream>>>((const char*)d_in[0], (const char*)d_in[1], (const char*)d_in[2], (char*)d_out WS_ARG);
}
```

```cpp
#define NTHR 512
#include <hip/hip_runtime.h>
#include <hip/hip_bf16.h>
#define LDSB 0
#define NBLK 256
__device__ __forceinline__ float bf2f(unsigned short b) { return __uint_as_float((unsigned)b << 16); }
__global__ __launch_bounds__(NTHR) void naive(const unsigned short* __restrict__ Q, const unsigned short* __restrict__ K, const unsigned short* __restrict__ V, __hip_bfloat16* __restrict__ O) {
  const unsigned long long t0 = __builtin_amdgcn_s_memrealtime();
  const int lane = threadIdx.x & 63, nw = NBLK * (NTHR / 64), gw = blockIdx.x * (NTHR / 64) + (threadIdx.x >> 6);
  for (int row = gw; row < 81920; row += nw) {
    const int hh = row / 320, kv = hh / 4;
    const unsigned short* q = Q + (size_t)row * 128; const unsigned short* k = K + (size_t)kv * 40960; const unsigned short* v = V + (size_t)kv * 40960;
    float s[5]; float m = -1e30f;
    for (int j = 0; j < 5; ++j) { const unsigned short* kr = k + (j * 64 + lane) * 128; float a = 0;
      for (int d = 0; d < 128; d += 2) { unsigned qq = *(const unsigned*)(q + d), kk = *(const unsigned*)(kr + d); a += bf2f(qq & 0xffff) * bf2f(kk & 0xffff) + bf2f(qq >> 16) * bf2f(kk >> 16); }
      s[j] = a * 0.08838834764831844f; m = fmaxf(m, s[j]); }
    for (int o = 32; o; o >>= 1) m = fmaxf(m, __shfl_xor(m, o));
    float l = 0; for (int j = 0; j < 5; ++j) { s[j] = __expf(s[j] - m); l += s[j]; }
    for (int o = 32; o; o >>= 1) l += __shfl_xor(l, o);
    float o0 = 0, o1 = 0;
    for (int j = 0; j < 5; ++j) for (int i = 0; i < 64; ++i) { float p = __shfl(s[j], i); unsigned vv = *(const unsigned*)(v + (j * 64 + i) * 128 + lane * 2); o0 += p * bf2f(vv & 0xffff); o1 += p * bf2f(vv >> 16); }
    O[(size_t)row * 128 + lane * 2] = __float2bfloat16(o0 / l); O[(size_t)row * 128 + lane * 2 + 1] = __float2bfloat16(o1 / l);
  }
  __builtin_amdgcn_fence(__ATOMIC_RELEASE, "agent"); asm volatile("s_waitcnt vmcnt(0)" ::: "memory");
  while (__builtin_amdgcn_s_memrealtime() - t0 < 300000ull) __builtin_amdgcn_s_sleep(1);
}
extern "C" void kernel_launch(void* const* d_in, const int* in_sizes, int n_in, void* d_out, int out_size, void* d_ws, size_t ws_size, hipStream_t stream) {
  naive<<<dim3(NBLK), dim3(NTHR), 0, stream>>>((const unsigned short*)d_in[0], (const unsigned short*)d_in[1], (const unsigned short*)d_in[2], (__hip_bfloat16*)d_out);
}
```

```cpp
#include <hip/hip_runtime.h>
#include <hip/hip_bf16.h>

typedef __bf16   bf16x8 __attribute__((ext_vector_type(8)));
typedef __bf16   bf16x2 __attribute__((ext_vector_type(2)));
typedef short    s16x4  __attribute__((ext_vector_type(4)));
typedef float    f32x16 __attribute__((ext_vector_type(16)));
typedef float    f32x4  __attribute__((ext_vector_type(4)));
typedef float    f32x2  __attribute__((ext_vector_type(2)));
typedef unsigned u32x4  __attribute__((ext_vector_type(4)));
#define DI __device__ __forceinline__
#define LDS_AS __attribute__((address_space(3)))

constexpr int NB = 8, HQ = 32, HKV = 8, SEQ = 320, HD = 128;
constexpr unsigned HEAD_BYTES = SEQ * HD * 2;
constexpr unsigned VOFF = HEAD_BYTES;
constexpr float CSC = 0.088388347648318440f * 1.4426950408889634f;
constexpr float THR_LOG2 = 4.0f;

DI unsigned cvtpk(float lo, float hi) {
  f32x2 v = {lo, hi}; bf16x2 b = __builtin_convertvector(v, bf16x2); return __builtin_bit_cast(unsigned, b);
}
DI float xhalf_max(float x) {
  auto r = __builtin_amdgcn_permlane32_swap(__float_as_uint(x), __float_as_uint(x), false, false);
  return fmaxf(__uint_as_float(r[0]), __uint_as_float(r[1]));
}
DI float xhalf_sum(float x) {
  auto r = __builtin_amdgcn_permlane32_swap(__float_as_uint(x), __float_as_uint(x), false, false);
  return __uint_as_float(r[0]) + __uint_as_float(r[1]);
}
DI void dma16(unsigned m0v, unsigned voff, u32x4 rsrc, unsigned soff) {
  asm volatile("s_mov_b32 m0, %0\n\ts_nop 0\n\tbuffer_load_dwordx4 %1, %2, %3 offen lds"
               :: "s"(m0v), "v"(voff), "s"(rsrc), "s"(soff) : "memory");
}
#define QLOAD(i) asm volatile("buffer_load_dwordx4 %0, %1, %2, %3 offen offset:" #i : "=v"(q[(i) / 32]) : "v"(qvoff), "s"(rq), "s"(qsoff) : "memory")
#define WAIT_Q(N) asm volatile("s_waitcnt vmcnt(" #N ")" : "+v"(q[0]), "+v"(q[1]), "+v"(q[2]), "+v"(q[3]), "+v"(q[4]), "+v"(q[5]), "+v"(q[6]), "+v"(q[7]) :: "memory")
#define WAITVM(N) asm volatile("s_waitcnt vmcnt(" #N ")" ::: "memory")
#define BARRIER() asm volatile("s_barrier" ::: "memory")
#define GAP() __builtin_amdgcn_sched_barrier(0)
#define PIN(x) asm volatile("" : "+v"(x))
#define MAX3(a, b, c) fmaxf(fmaxf(a, b), c)
#define ADV() do { c.kb0 += 8192; c.kb1 += 8192; c.vb += 8192; PIN(c.kb0); PIN(c.kb1); PIN(c.vb); } while (0)

constexpr int RING = 4, LA = 3;
struct St {
  f32x16 o[4];
  f32x16 s;
  float x[16];
  unsigned p[8];
  bf16x8 ring[RING];
  float mc, l0, alpha;
  float mx[2];
  int resc;
  unsigned kb0, kb1, vb;
};
DI bf16x8 tr_pair(const LDS_AS char* lds, unsigned a0, unsigned a1) {
  const s16x4 lo = __builtin_amdgcn_ds_read_tr16_b64_v4i16((LDS_AS s16x4*)(lds + a0));
  const s16x4 hi = __builtin_amdgcn_ds_read_tr16_b64_v4i16((LDS_AS s16x4*)(lds + a1));
  return __builtin_bit_cast(bf16x8, __builtin_shufflevector(lo, hi, 0, 1, 2, 3, 4, 5, 6, 7));
}
template <int G> DI void frag_read(St& c, const LDS_AS char* lds) {
  constexpr int g = G & 15; constexpr unsigned nxt = (G >> 4) * 8192u;
  if constexpr (G < 0) return;
  else if constexpr (g < 8) {
    constexpr unsigned off = nxt + 4096u * (g >> 2) + 512u * (g & 3);
    c.ring[G % RING] = tr_pair(lds, c.vb + off, c.vb + off + 2048u);
  } else {
    constexpr int st = g - 8;
    c.ring[G % RING] = *(const LDS_AS bf16x8*)(lds + (((st & 1) ? c.kb1 : c.kb0) + nxt + 512u * (st >> 1)));
  }
}
#define F_(e) do { c.x[e] = fmaf(c.s[e], CSC, c.mc); PIN(c.x[e]); } while (0)
#define E_(e) do { c.x[e] = __builtin_amdgcn_exp2f(c.x[e]); PIN(c.x[e]); } while (0)
#define A_(e) do { c.l0 += c.x[e]; PIN(c.l0); } while (0)
DI float MAXA(float a, float b, float c) { float r; asm("v_max3_f32 %0, %1, %2, %3" : "=v"(r) : "v"(a), "v"(b), "v"(c)); return r; }
#define C_(e) do { c.p[e] = cvtpk(c.x[2 * (e)], c.x[2 * (e) + 1]); PIN(c.p[e]); } while (0)
template <int V, bool DEFER = false> DI void sm_slice(St& c) {
  if constexpr (V == 0) { A_(12); C_(6); A_(13); C_(7); A_(14); }
  if constexpr (V == 1) A_(15);
  if constexpr (V >= 1 && V <= 4) { constexpr int e = 4 * (V - 1); F_(e); F_(e + 1); F_(e + 2); F_(e + 3); }
  if constexpr (V == 2) { { float r; asm("v_max3_f32 %0, 4.0, %1, %2" : "=v"(r) : "v"(c.x[0]), "v"(c.x[1])); c.mx[0] = r; } }
  if constexpr (V == 3) { c.mx[1] = MAXA(c.x[2], c.x[3], c.x[4]); c.mx[0] = MAXA(c.mx[0], c.x[5], c.x[6]); }
  if constexpr (V == 4) { c.mx[1] = MAXA(c.mx[1], c.x[7], c.x[8]); }
  if constexpr (V == 5) { c.mx[0] = MAXA(c.mx[0], c.x[9], c.x[10]); c.mx[1] = MAXA(c.mx[1], c.x[11], c.x[12]); E_(0); E_(1); }
  if constexpr (V == 6) { c.mx[0] = MAXA(c.mx[0], c.x[13], c.x[14]); c.mx[0] = MAXA(c.mx[0], c.mx[1], c.x[15]); E_(2); E_(3); }
  if constexpr (V == 7) {
    E_(4); E_(5);
    if (__builtin_expect(__builtin_amdgcn_ballot_w64(c.mx[0] > THR_LOG2) != 0, 0)) {
      float mx = c.s[0];
#pragma unroll
      for (int e = 1; e < 16; ++e) mx = fmaxf(mx, c.s[e]);
      mx = xhalf_max(mx);
      const float mcn = fminf(c.mc, -mx * CSC);
      const float alpha = __builtin_amdgcn_exp2f(mcn - c.mc);
      c.mc = mcn;
      c.l0 *= alpha;
      if constexpr (DEFER) { c.alpha = alpha; c.resc = 1; }
      else {
#pragma unroll
        for (int t = 0; t < 4; ++t)
#pragma unroll
          for (int r = 0; r < 16; ++r) c.o[t][r] *= alpha;
      }
#pragma unroll
      for (int e = 0; e < 16; ++e) c.x[e] = fmaf(c.s[e], CSC, c.mc);
#pragma unroll
      for (int e = 0; e < 6; ++e) c.x[e] = __builtin_amdgcn_exp2f(c.x[e]);
    }
  }
  if constexpr (V == 8)  { A_(0); E_(6); A_(1); E_(7); }
  if constexpr (V == 9)  { E_(8); E_(9); C_(0); A_(2); }
  if constexpr (V == 10) { E_(10); E_(11); A_(3); C_(1); }
  if constexpr (V == 11) { A_(4); E_(12); A_(5); E_(13); }
  if constexpr (V == 12) { E_(14); E_(15); C_(2); A_(6); }
  if constexpr (V == 13) { A_(7); C_(3); A_(8); C_(4); A_(9); }
  if constexpr (V == 14) { A_(10); C_(5); A_(11); }
}
DI bf16x8 pk4(const unsigned* p) { return __builtin_bit_cast(bf16x8, u32x4{p[0], p[1], p[2], p[3]}); }

template <bool QK, bool NPV, bool NQK> constexpr int ahead(int a) {
  const int last = QK ? 16 : 8;
  if (a < last) return a;
  const int k = a - last;
  if (NPV) return (k < 8 || NQK) ? 16 + k : -1;
  if (NQK) return k < 8 ? 24 + k : -1;
  return -1;
}
constexpr int pad_next(int n) { return n < 8 ? n + 4 : n < 12 ? 100 : n < 28 ? ((n & 3) == 3 ? 102 : n + 1) : ((n - 28) % 3 == 2 ? 103 : n + 1); }
#define PAD(n) asm volatile(".Lgqa_pf%c0:\n\ts_cbranch_execz .Lgqa_pf%c1" :: "i"(n), "i"(pad_next(n)))
#define WALK4(sel, base, stride, ret) do { unsigned long long sv_; \
    asm volatile("s_mov_b64 %0, exec\n\ts_mov_b64 exec, 0\n\t" \
                 "s_cmp_eq_u32 %1, 1\n\ts_cbranch_scc1 .Lgqa_pf%c3\n\t" \
                 "s_cmp_eq_u32 %1, 2\n\ts_cbranch_scc1 .Lgqa_pf%c4\n\t" \
                 "s_cmp_eq_u32 %1, 3\n\ts_cbranch_scc1 .Lgqa_pf%c5\n\t" \
                 "s_branch .Lgqa_pf%c2\n" \
                 ".Lgqa_pf%c6:\n\ts_mov_b64 exec, %0" \
                 : "=&s"(sv_) : "s"(sel), "i"(base), "i"((base) + (stride)), "i"((base) + 2 * (stride)), "i"((base) + 3 * (stride)), "i"(ret) : "scc"); } while (0)
template <bool PV, int SM, bool QK, bool NPV, bool NQK, int PB, int PE, int I> DI void slot(St& c, const bf16x8 (&q)[8], const LDS_AS char* lds, bf16x8& pa, bf16x8& pb) {
  constexpr int first = PV ? 0 : 8, last = QK ? 16 : 8, n = last - first, g = first + I;
  if constexpr (g == 4) pb = pk4(c.p + 4);
  if constexpr (g < 4)       c.o[g & 3] = __builtin_amdgcn_mfma_f32_32x32x16_bf16(c.ring[g % RING], pa, c.o[g & 3], 0, 0, 0);
  else if constexpr (g < 8)  c.o[g & 3] = __builtin_amdgcn_mfma_f32_32x32x16_bf16(c.ring[g % RING], pb, c.o[g & 3], 0, 0, 0);
  else if constexpr (g == 8) c.s = __builtin_amdgcn_mfma_f32_32x32x16_bf16(c.ring[g % RING], q[0], f32x16{}, 0, 0, 0);
  else                       c.s = __builtin_amdgcn_mfma_f32_32x32x16_bf16(c.ring[g % RING], q[g - 8], c.s, 0, 0, 0);
  if constexpr (g & 1) { frag_read<ahead<QK, NPV, NQK>(g + LA + 1)>(c, lds); frag_read<ahead<QK, NPV, NQK>(g + LA)>(c, lds); }
  if constexpr (!PV && SM == 0 && (I & 1) == 0) { c.o[I >> 1] = f32x16{}; PIN(c.o[I >> 1]); }
  if constexpr (SM == 1) {
    if constexpr (n == 16) sm_slice<I>(c); else { sm_slice<2 * I, true>(c); sm_slice<2 * I + 1, true>(c); }
  } else if constexpr (SM == 2) {
    E_(2 * I); E_(2 * I + 1);
    if constexpr (I >= 1 && I <= 6) { A_(2 * I - 2); C_(I - 1); A_(2 * I - 1); }
  } else if constexpr (SM == 3) {
    if constexpr (I == 0) sm_slice<0>(c);
    if constexpr (I == 1) A_(15);
  }
  if constexpr (PB >= 0 && (I % PE) == PE - 1) PAD(PB + I / PE);
  GAP();
}
template <bool PV, int SM, bool QK, bool NPV, bool NQK, int PB = -1, int PE = 4>
DI void step(St& c, const bf16x8 (&q)[8], const LDS_AS char* lds) {
  bf16x8 pa = pk4(c.p), pb = pa;
  if constexpr (SM == 1 && !QK) { c.alpha = 1.f; c.resc = 0; }
#define SL(I) slot<PV, SM, QK, NPV, NQK, PB, PE, I>(c, q, lds, pa, pb)
  SL(0); SL(1); SL(2); SL(3); SL(4); SL(5); SL(6); SL(7);
  if constexpr (PV && QK) { SL(8); SL(9); SL(10); SL(11); SL(12); SL(13); SL(14); SL(15); }
#undef SL
  if constexpr (SM == 1 && !QK) {
    if (__builtin_expect(c.resc, 0)) {
#pragma unroll
      for (int t = 0; t < 4; ++t)
#pragma unroll
        for (int r = 0; r < 16; ++r) c.o[t][r] *= c.alpha;
    }
  }
}
DI u32x4 pack8(const f32x16& o, int hf, float sc) {
  u32x4 w;
#pragma unroll
  for (int j = 0; j < 4; ++j) w[j] = cvtpk(o[8 * hf + 2 * j] * sc, o[8 * hf + 2 * j + 1] * sc);
  return w;
}

constexpr int RING16 = 8, LA16 = 5;
struct Sc {
  f32x4 o[8];
  f32x4 s[2];
  float x[8];
  unsigned p[4];
  bf16x8 ring[RING16];
  float mc, mthr, l0, alpha, mx0, mx1;
  int resc;
  unsigned kc[4], vbe, vbo;
};
DI float xrow_max(float x) {
  auto r = __builtin_amdgcn_permlane16_swap(__float_as_uint(x), __float_as_uint(x), false, false);
  return xhalf_max(fmaxf(__uint_as_float(r[0]), __uint_as_float(r[1])));
}
DI float xrow_sum(float x) {
  auto r = __builtin_amdgcn_permlane16_swap(__float_as_uint(x), __float_as_uint(x), false, false);
  return xhalf_sum(__uint_as_float(r[0]) + __uint_as_float(r[1]));
}
template <int G> DI void frag_read16(Sc& c, const LDS_AS char* lds) {
  constexpr int g = G & 15; constexpr unsigned nxt = (G >> 4) * 8192u;
  if constexpr (G < 0) return;
  else if constexpr (g < 8) {
    constexpr unsigned off = nxt + 512u * (g >> 1);
    const unsigned b = (g & 1) ? c.vbo : c.vbe;
    c.ring[G % RING16] = tr_pair(lds, b + off, b + off + 4096u);
  } else {
    constexpr int st = (g - 8) >> 1, a = g & 1;
    c.ring[G % RING16] = *(const LDS_AS bf16x8*)(lds + (c.kc[st] + nxt + 4096u * a));
  }
}
#define SV(e) c.s[(e) >> 2][(e) & 3]
#define F16_(e) do { c.x[e] = fmaf(SV(e), CSC, c.mc); PIN(c.x[e]); } while (0)
template <int V> DI void sm_slice16(Sc& c) {
  if constexpr (V == 2) { F16_(0); F16_(1); c.mx0 = MAX3(c.mthr, SV(0), SV(1)); }
  if constexpr (V == 3) { F16_(2); F16_(3); c.mx1 = MAX3(SV(2), SV(3), SV(4)); }
  if constexpr (V == 4) { F16_(4); F16_(5); c.mx0 = MAX3(c.mx0, SV(5), SV(6)); }
  if constexpr (V == 5) { F16_(6); F16_(7); }
  if constexpr (V == 6) {
    const float mxt = MAX3(c.mx0, c.mx1, SV(7));
    if (__builtin_expect(__builtin_amdgcn_ballot_w64(mxt > c.mthr) != 0, 0)) {
      float mx = SV(0);
#pragma unroll
      for (int e = 1; e < 8; ++e) mx = fmaxf(mx, SV(e));
      mx = xrow_max(mx);
      const float mcn = fminf(c.mc, -mx * CSC);
      c.alpha = __builtin_amdgcn_exp2f(mcn - c.mc);
      c.mc = mcn; c.mthr = fmaxf(c.mthr, mx + THR_LOG2 / CSC);
      c.l0 *= c.alpha; c.resc = 1;
#pragma unroll
      for (int e = 0; e < 8; ++e) c.x[e] = fmaf(SV(e), CSC, c.mc);
    }
  }
  if constexpr (V >= 7 && V <= 10) { E_(2 * (V - 7)); E_(2 * (V - 7) + 1); }
  if constexpr (V >= 11 && V <= 14) { A_(2 * (V - 11)); C_(V - 11); A_(2 * (V - 11) + 1); }
}
template <bool PV, int SM, bool QK, bool NPV, bool NQK, int PB, int PE, int I> DI void slot16(Sc& c, const bf16x8 (&q)[4], const LDS_AS char* lds, const bf16x8& pa) {
  constexpr int first = PV ? 0 : 8, last = QK ? 16 : 8, n = last - first, g = first + I;
  if constexpr (g < 8)        c.o[g] = __builtin_amdgcn_mfma_f32_16x16x32_bf16(c.ring[g % RING16], pa, c.o[g], 0, 0, 0);
  else if constexpr (g < 10)  c.s[g & 1] = __builtin_amdgcn_mfma_f32_16x16x32_bf16(c.ring[g % RING16], q[0], f32x4{}, 0, 0, 0);
  else                        c.s[g & 1] = __builtin_amdgcn_mfma_f32_16x16x32_bf16(c.ring[g % RING16], q[(g - 8) >> 1], c.s[g & 1], 0, 0, 0);
  if constexpr (g & 1) { frag_read16<ahead<QK, NPV, NQK>(g + LA16 + 1)>(c, lds); frag_read16<ahead<QK, NPV, NQK>(g + LA16)>(c, lds); }
  if constexpr (SM == 1) {
    if constexpr (n == 16) sm_slice16<I>(c); else { sm_slice16<2 * I>(c); sm_slice16<2 * I + 1>(c); }
  } else if constexpr (SM == 2) {
    sm_slice16<7 + I>(c);
  }
  if constexpr (PB >= 0 && (I % PE) == PE - 1) PAD(PB + I / PE);
  GAP();
}
template <bool PV, int SM, bool QK, bool NPV, bool NQK, int PB = -1, int PE = 4>
DI void step16(Sc& c, const bf16x8 (&q)[4], const LDS_AS char* lds) {
  const bf16x8 pa = pk4(c.p);
#define SL(I) slot16<PV, SM, QK, NPV, NQK, PB, PE, I>(c, q, lds, pa)
  SL(0); SL(1); SL(2); SL(3); SL(4); SL(5); SL(6); SL(7);
  if constexpr (PV && QK) { SL(8); SL(9); SL(10); SL(11); SL(12); SL(13); SL(14); SL(15); }
#undef SL
  if constexpr (SM == 1) {
    if (__builtin_expect(c.resc, 0)) {
#pragma unroll
      for (int t = 0; t < 8; ++t)
#pragma unroll
        for (int r = 0; r < 4; ++r) c.o[t][r] *= c.alpha;
      c.resc = 0;
    }
  }
}
#define ADV16() do { _Pragma("unroll") for (int i_ = 0; i_ < 4; ++i_) { c.kc[i_] += 8192; PIN(c.kc[i_]); } c.vbe += 8192; c.vbo += 8192; PIN(c.vbe); PIN(c.vbo); } while (0)

constexpr int ST_AUX = 19;
constexpr int NT0 = 5;
constexpr unsigned AMASK = (1u << 2) | (1u << 4) | (1u << 6);
#define LANE_DEFS \
    unsigned lane; asm volatile("v_mbcnt_lo_u32_b32 %0, -1, 0\n\tv_mbcnt_hi_u32_b32 %0, -1, %0" : "=v"(lane));

__global__ __launch_bounds__(768, 3)
void gqa_attn_kernel(const char* __restrict__ Q, const char* __restrict__ K, const char* __restrict__ V, char* __restrict__ O) {
  __shared__ __attribute__((aligned(16))) char smem[2 * HEAD_BYTES];
  const LDS_AS char* lds = (const LDS_AS char*)smem;
  const unsigned ldsb = (unsigned)(size_t)(LDS_AS char*)smem;
  typedef unsigned long long u64x4 __attribute__((ext_vector_type(4)));
  u64x4 ka;
  asm volatile("s_load_dwordx8 %0, %1, 0x0" : "=s"(ka) : "s"(__builtin_amdgcn_kernarg_segment_ptr()) : "memory");
  const unsigned wave = __builtin_amdgcn_readfirstlane(threadIdx.x >> 6);
  unsigned pf;
  { unsigned long long pc; unsigned len;
    asm volatile(".Lgqa_begin:\n\ts_getpc_b64 %0\n\ts_mov_b32 %1, .Lgqa_pf39-.Lgqa_begin\n\ts_max_u32 %1, %1, .Lgqa_pf27-.Lgqa_begin" : "=s"(pc), "=s"(len) :: "scc");
    const unsigned o = (threadIdx.x - 640u) * 128u;
    asm volatile("global_load_dword %0, %1, %2" : "=v"(pf) : "v"(o < len ? o : 0u), "s"(pc) : "memory"); }
#define PF_DONE() asm volatile("" :: "v"(pf))
  const unsigned bid = blockIdx.x, xcd = bid & 7, idx = bid >> 3;
  const unsigned grp = xcd * 8 + (idx >> 2);
  const unsigned qh = grp * 4 + (idx & 3);
  auto mk = [](const char* p) { u32x4 r; const unsigned long long a = (unsigned long long)p;
    r[0] = (unsigned)a; r[1] = (unsigned)(a >> 32) & 0xffffu; r[2] = HEAD_BYTES; r[3] = 0x00020000u; return r; };

  const bool isC = wave >= 8, isVw = isC && (wave & 1);
  const unsigned slot = (wave & 1) * VOFF + (8 + (wave >> 1)) * 8192;
  unsigned d_m0 = ldsb + (isC ? (wave & 1) * VOFF + (wave & 2) * 512 : slot);
  unsigned d_so = isC ? (wave & 2) * 64 : wave * 8192;
  const unsigned d_vx = isVw ? 0u : 32u;
  unsigned d_vo;
  { const unsigned ln = threadIdx.x & 63, row7 = (ln >> 2) & 7;
    d_vo = row7 * 256 + (ln >> 5) * 64 + (((ln & 3) ^ ((row7 >> 2) << (isVw ? 1 : 0))) << 4); PIN(d_vo); }
  int n0 = isC ? 4 * NT0 : (wave < 4 ? 8 : 0);
  unsigned dm = isC ? 0u : 1024u - 8192u, ds = isC ? 0u : 128u - 8192u;
  unsigned long long qoff = (unsigned long long)qh * HEAD_BYTES, doff = (unsigned long long)(isC ? grp : qh) * HEAD_BYTES;
  asm volatile("s_waitcnt lgkmcnt(0)" : "+s"(ka), "+s"(n0), "+s"(dm), "+s"(ds), "+s"(qoff), "+s"(doff), "+s"(d_m0), "+s"(d_so) :: "memory");
  const u32x4 rd = mk((const char*)((isC ? (isVw ? ka[2] : ka[1]) : ka[0]) + doff));
  {
    const int n = n0;
#pragma unroll 1
    for (int i = 0; i < n; ++i) {
      dma16(d_m0, d_vo, rd, d_so); d_m0 += 2048; d_so += 2048; d_vo ^= d_vx;
      if (i == 3) { d_m0 += dm; d_so += ds; }
    }
  }
  const u32x4 rq = mk((const char*)(ka[0] + qoff));
  const __amdgpu_buffer_rsrc_t ro = __builtin_amdgcn_make_buffer_rsrc((char*)(ka[3] + qoff), 0, HEAD_BYTES, 0x00020000);

  if (isC) {
    LANE_DEFS
    {
      auto kv_run = [&](int n) {
#pragma unroll 1
        for (int i = 0; i < n; ++i) { dma16(d_m0, d_vo, rd, d_so); d_m0 += 2048; d_so += 2048; d_vo ^= d_vx; }
      };
      asm volatile("s_waitcnt vmcnt(%0)" :: "i"(4 * (NT0 - 3)) : "memory");
      BARRIER();
      kv_run(4 * (7 - NT0));
      WAITVM(8);
      BARRIER();
      kv_run(12);
      WAITVM(12);
      BARRIER();
    }
    const unsigned l15 = lane & 15, g = lane >> 4;
    bf16x8 q[4];
    { const unsigned qvoff = l15 * 256 + g * 64, qsoff = 65536 + (wave - 8) * 4096;
      asm volatile(
                   "buffer_load_dwordx4 %0, %4, %5, %6 offen\n\tbuffer_load_dwordx4 %1, %4, %5, %6 offen offset:16\n\t"
                   "buffer_load_dwordx4 %2, %4, %5, %6 offen offset:32\n\tbuffer_load_dwordx4 %3, %4, %5, %6 offen offset:48"
                   "\n\ts_waitcnt vmcnt(4)\n\ts_barrier"
                   : "=&v"(q[0]), "=&v"(q[1]), "=&v"(q[2]), "=&v"(q[3]) : "v"(qvoff), "s"(rq), "s"(qsoff) : "memory"); }
    asm volatile("s_waitcnt vmcnt(0)" : "+v"(q[0]), "+v"(q[1]), "+v"(q[2]), "+v"(q[3]) :: "memory");
    PF_DONE();
    Sc c;
    const unsigned kc0 = 2048 * (l15 >> 3) + 64 * (l15 & 7) + 512 * g + (((l15 >> 2) & 3) << 4);
#pragma unroll
    for (int st = 0; st < 4; ++st) { c.kc[st] = kc0 ^ (unsigned)(st << 4); PIN(c.kc[st]); }
    c.vbe = VOFF - 16384 + 2048 * (g >> 1) + 64 * (4 * (g & 1) + ((lane >> 2) & 3)) + 32 * (g & 1) + 8 * (lane & 3); c.vbo = c.vbe ^ 32u; PIN(c.vbe); PIN(c.vbo);
    frag_read16<9>(c, lds); frag_read16<8>(c, lds); frag_read16<11>(c, lds); frag_read16<10>(c, lds);
    frag_read16<13>(c, lds); frag_read16<12>(c, lds);
#pragma unroll
    for (int t = 0; t < 8; ++t) c.o[t] = f32x4{};
#pragma unroll
    for (int e = 0; e < 4; ++e) c.p[e] = 0;
    c.l0 = 0.f; c.alpha = 1.f; c.resc = 0;
    PAD(28);
    GAP();
    step16<false, 0, true, false, true, 29, 8>(c, q, lds);
    ADV16();
    {
      float mx = SV(0);
#pragma unroll
      for (int e = 1; e < 8; ++e) mx = fmaxf(mx, SV(e));
      mx = xrow_max(mx); c.mc = -mx * CSC; c.mthr = mx + THR_LOG2 / CSC;
#pragma unroll
      for (int e = 0; e < 8; ++e) { c.x[e] = fmaf(SV(e), CSC, c.mc); PIN(c.x[e]); }
      GAP(); PAD(37); GAP();
    }
    step16<false, 2, true, true, true, 30, 8>(c, q, lds);
    ADV16();
#pragma unroll 1
    for (int j = 1; j <= 8; ++j) {
      step16<true, 1, true, true, true, 31, 4>(c, q, lds);
      ADV16();
    }
    step16<true, 1, false, true, false, 35, 4>(c, q, lds);
    c.vbe += 8192; c.vbo += 8192; PIN(c.vbe); PIN(c.vbo);
    step16<true, 0, false, false, false>(c, q, lds);
    {
      const float sc = __builtin_amdgcn_rcpf(xrow_sum(c.l0));
      u32x4 w[4];
#pragma unroll
      for (int T = 0; T < 4; ++T) {
        const unsigned a0 = cvtpk(c.o[2 * T][0] * sc, c.o[2 * T][1] * sc), a1 = cvtpk(c.o[2 * T][2] * sc, c.o[2 * T][3] * sc);
        const unsigned b0 = cvtpk(c.o[2 * T + 1][0] * sc, c.o[2 * T + 1][1] * sc), b1 = cvtpk(c.o[2 * T + 1][2] * sc, c.o[2 * T + 1][3] * sc);
        auto r0 = __builtin_amdgcn_permlane16_swap(a0, b0, false, false);
        auto r1 = __builtin_amdgcn_permlane16_swap(a1, b1, false, false);
        w[T] = u32x4{r0[0], r1[0], r0[1], r1[1]};
        PIN(w[T]);
        if (T == 0) { GAP(); PAD(38); GAP(); }
      }
      GAP(); PAD(39); GAP();
      const unsigned rowp = 65536 + (wave - 8) * 4096 + (l15 & 7) * 256 + (l15 >> 3) * 64 + (g & 1) * 32 + (g >> 1) * 16;
#pragma unroll
      for (int tp = 0; tp < 2; ++tp) {
        u32x4 x, y;
#pragma unroll
        for (int e = 0; e < 4; ++e) {
          x[e] = __builtin_amdgcn_update_dpp(w[2 * tp][e], w[2 * tp + 1][e], 0x128, 0xf, 0xc, false);
          y[e] = __builtin_amdgcn_update_dpp(w[2 * tp + 1][e], w[2 * tp][e], 0x128, 0xf, 0x3, false);
        }
        __builtin_amdgcn_raw_buffer_store_b128(x, ro, rowp + 128 * tp, 0, ST_AUX);
        __builtin_amdgcn_raw_buffer_store_b128(y, ro, rowp + 128 * tp + 2048, 0, ST_AUX);
      }
    }
    __builtin_amdgcn_endpgm();
  } else {
    LANE_DEFS
    const unsigned r32 = lane & 31, h = lane >> 5;
    const bool isA = wave < 4;
    bf16x8 q[8];
    const unsigned kbase0 = 2048 * (r32 >> 3) + 64 * (r32 & 7) + ((h ^ ((r32 >> 2) & 3)) << 4);
    if (isA) {
      BARRIER();
      WAITVM(0);
#pragma unroll
      for (int st = 0; st < 8; ++st) q[st] = *(const LDS_AS bf16x8*)(lds + (((st & 1) ? (kbase0 ^ 32u) : kbase0) + slot + 512u * (st >> 1)));
      __builtin_amdgcn_s_setprio(2);
    } else {
      const unsigned qvoff = r32 * 256 + h * 16;
      WALK4(wave - 4, 0, 1, 100);
      WALK4(wave - 4, 12, 4, 102);
      WALK4(wave - 4, 28, 3, 103);
      BARRIER();
      BARRIER();
      BARRIER();
      { const unsigned qsoff = wave * 8192;
        QLOAD(0); QLOAD(32); QLOAD(64); QLOAD(96); QLOAD(128); QLOAD(160); QLOAD(192); QLOAD(224); }
      BARRIER();
      __builtin_amdgcn_s_setprio(1);
    }
    PAD(2);
    const unsigned syncmask = isA ? AMASK : 0u;
    St c;
    c.kb0 = kbase0; c.kb1 = c.kb0 ^ 32u;
    c.vb = VOFF - 16384 + 64 * (4 * h + ((lane >> 2) & 3)) + 32 * (((lane >> 4) & 1) ^ h) + 16 * (lane & 1) + 8 * ((lane >> 1) & 1);
    PIN(c.kb0); PIN(c.kb1); PIN(c.vb);
    frag_read<9>(c, lds); frag_read<8>(c, lds); frag_read<11>(c, lds); frag_read<10>(c, lds);
    WAIT_Q(0);
    PF_DONE();
#pragma unroll
    for (int e = 0; e < 8; ++e) c.p[e] = 0;
    c.l0 = 0.f;
    PAD(3);
    GAP();
    step<false, 0, true, false, true, 4, 8>(c, q, lds);
    ADV();
    {
      float mx = c.s[0];
#pragma unroll
      for (int r = 1; r < 16; ++r) mx = fmaxf(mx, c.s[r]);
      mx = xhalf_max(mx); c.mc = -mx * CSC;
      GAP(); PAD(5); GAP();
#pragma unroll
      for (int e = 0; e < 16; ++e) { c.x[e] = fmaf(c.s[e], CSC, c.mc); PIN(c.x[e]); }
      GAP(); PAD(0); GAP();
    }
    step<false, 2, true, true, true, 6, 4>(c, q, lds);
    ADV();
    GAP(); PAD(1); GAP();
#pragma unroll 1
    for (int j = 1; j <= 8; ++j) {
      if ((syncmask >> j) & 1) BARRIER();
      step<true, 1, true, true, true, 8, 4>(c, q, lds);
      ADV();
    }
    PAD(12);
    step<true, 1, false, true, false, 13, 2>(c, q, lds);
    c.vb += 8192; PIN(c.vb);
    step<true, 3, false, false, false, 17, 4>(c, q, lds);
    PAD(19);
    {
      const float sc = __builtin_amdgcn_rcpf(xhalf_sum(c.l0));
      unsigned ln; asm volatile("v_mbcnt_lo_u32_b32 %0, -1, 0\n\tv_mbcnt_hi_u32_b32 %0, -1, %0" : "=v"(ln));
      const unsigned rowp = wave * 8192 + (ln & 7) * 256 + ((ln >> 3) & 1) * 64 + ((ln >> 4) & 1) * 32 + (ln >> 5) * 16;
#pragma unroll
      for (int tp = 0; tp < 2; ++tp) {
        u32x4 w[2][2];
#pragma unroll
        for (int u = 0; u < 2; ++u) {
          w[u][0] = pack8(c.o[2 * tp + u], 0, sc); w[u][1] = pack8(c.o[2 * tp + u], 1, sc);
#pragma unroll
          for (int e = 0; e < 4; ++e) { auto r = __builtin_amdgcn_permlane16_swap(w[u][0][e], w[u][1][e], false, false); w[u][0][e] = r[0]; w[u][1][e] = r[1]; }
          PIN(w[u][0]); PIN(w[u][1]);
          GAP(); PAD(20 + 4 * tp + u); GAP();
        }
#pragma unroll
        for (int k = 0; k < 2; ++k) {
          u32x4 x, y;
#pragma unroll
          for (int e = 0; e < 4; ++e) {
            x[e] = __builtin_amdgcn_update_dpp(w[0][k][e], w[1][k][e], 0x128, 0xf, 0xc, false);
            y[e] = __builtin_amdgcn_update_dpp(w[1][k][e], w[0][k][e], 0x128, 0xf, 0x3, false);
          }
          __builtin_amdgcn_raw_buffer_store_b128(x, ro, rowp + 128 * tp + 4096 * k, 0, ST_AUX);
          __builtin_amdgcn_raw_buffer_store_b128(y, ro, rowp + 128 * tp + 4096 * k + 2048, 0, ST_AUX);
          GAP(); PAD(22 + 4 * tp + k); GAP();
        }
      }
    }
    __builtin_amdgcn_endpgm();
  }
}

extern "C" void kernel_launch(void* const* d_in, const int* in_sizes, int n_in,
                              void* d_out, int out_size, void* d_ws, size_t ws_size,
                              hipStream_t stream) {
  gqa_attn_kernel<<<dim3(NB * HQ), dim3(768), 0, stream>>>((const char*)d_in[0], (const char*)d_in[1], (const char*)d_in[2], (char*)d_out);
}
```
